```python
import torch

def _softmax(x, axis=-1):
    return torch.softmax(x, dim=axis)
def _gelu(x):
    return torch.nn.functional.gelu(x, approximate="tanh")
def _silu(x):
    return torch.nn.functional.silu(x)
def _sigmoid(x):
    return torch.sigmoid(x)
def _layernorm(x, axis=-1, eps=1e-5):
    m = x.mean(dim=axis, keepdim=True)
    v = x.var(dim=axis, keepdim=True, unbiased=False)
    return (x - m) / torch.sqrt(v + eps)
def _scatter_add(tgt, idx, src, axis=0):
    return tgt.clone().index_add_(axis, idx, src)
def _segment_sum(src, seg, n):
    out = torch.zeros((n,) + src.shape[1:], dtype=src.dtype)
    return out.index_add_(0, seg, src)
def _pad(x, pw):
    flat = tuple(v for pair in reversed(pw) for v in pair)
    return torch.nn.functional.pad(x, flat)

kernel_name = 'concat_heads__linear_attn_block__linear_kernel_hedgehog__prompt'

def setup_inputs():

    _w = (lambda a: (lambda t: t.to(torch.bfloat16) if t.is_floating_point() else t)(torch.as_tensor(a)))
    def _A_setup():
        B, S, D = 1, 2048, 576
        return {
            "x": _w((lambda *s: torch.randn(*s, dtype=torch.bfloat16) * 0.05)(B, S, D)),
            "wqkv": _w((lambda *s: torch.randn(*s, dtype=torch.bfloat16) * 0.8)(D, 3 * D)),
            "wo": _w((lambda *s: torch.randn(*s, dtype=torch.bfloat16) * 0.2)(D, D)),
        }
    _A_in = _A_setup()

    def _B_setup():
        B, S, D, H = 1, 2048, 576, 32
        Dh = D // H
        return {
            "x": _w((lambda *s: torch.randn(*s, dtype=torch.bfloat16) * 0.4)(B, S, D)),
            "wqkv": _w((lambda *s: torch.randn(*s, dtype=torch.bfloat16) * 0.4)(D, 3 * D)),
            "w_phi": _w((lambda *s: torch.randn(*s, dtype=torch.bfloat16) * 0.4)(H, Dh, Dh) * (Dh ** -0.5)),
            "wo": _w((lambda *s: torch.randn(*s, dtype=torch.bfloat16) * 0.05)(D, D)),
        }
    _B_in = _B_setup()

    return {
        "A_x": _A_in["x"],
        "A_wqkv": _A_in["wqkv"],
        "A_wo": _A_in["wo"],
        "B_x": _B_in["x"],
        "B_wqkv": _B_in["wqkv"],
        "B_w_phi": _B_in["w_phi"],
        "B_wo": _B_in["wo"],
    }

def reference(A_x, A_wqkv, A_wo, B_x, B_wqkv, B_w_phi, B_wo):
    def _A_ref(x, wqkv, wo):
        B, S, D = x.shape
        H = 32; Dh = D // H
        qkv = (lambda x, *d: x.permute(*d))((x @ wqkv).reshape(B, S, 3, H, Dh), 2, 0, 3, 1, 4)
        q, k, v = qkv[0], qkv[1], qkv[2]
        phi = lambda t: torch.where(t > 0, t + 1, torch.exp(t)) + 1e-6
        pq, pk = phi(q), phi(k)
        kv = torch.einsum("bhsd,bhse->bhde", pk, v)
        z = torch.einsum("bhsd->bhd", pk)
        num = torch.einsum("bhqd,bhde->bhqe", pq, kv)
        den = torch.einsum("bhqd,bhd->bhq", pq, z)[..., None] + 1e-6
        o = num / den
        return (lambda x, *d: x.permute(*d))(o, 0, 2, 1, 3).reshape(B, S, D) @ wo

    def _B_ref(x, wqkv, w_phi, wo):
        B, S, D = x.shape
        H = 32; Dh = D // H
        qkv = (lambda x, *d: x.permute(*d))((x @ wqkv).reshape(B, S, 3, H, Dh), 2, 0, 3, 1, 4)
        q, k, v = qkv[0], qkv[1], qkv[2]
        pq = torch.einsum("bhsd,hdf->bhsf", q, w_phi)
        pk = torch.einsum("bhsd,hdf->bhsf", k, w_phi)
        phi_q = torch.concatenate([_softmax(pq, axis=-1), _softmax(-pq, axis=-1)], axis=-1)
        phi_k = torch.concatenate([_softmax(pk, axis=-1), _softmax(-pk, axis=-1)], axis=-1)
        kv = torch.einsum("bhsf,bhsd->bhfd", phi_k, v)
        z  = torch.einsum("bhsf->bhf", phi_k)
        num = torch.einsum("bhqf,bhfd->bhqd", phi_q, kv)
        den = torch.einsum("bhqf,bhf->bhq", phi_q, z)[..., None] + 1e-6
        o = num / den
        return (lambda x, *d: x.permute(*d))(o, 0, 2, 1, 3).reshape(B, S, D) @ wo

    a = _A_ref(A_x, A_wqkv, A_wo)
    b = _B_ref(B_x, B_wqkv, B_w_phi, B_wo)
    return torch.concatenate([a, b], axis=-1)
```

```cpp
#include <hip/hip_runtime.h>
#include <hip/hip_bf16.h>
typedef __attribute__((ext_vector_type(8))) short bf16x8;
typedef __attribute__((ext_vector_type(4))) float f32x4;
typedef unsigned short u16;
__device__ __forceinline__ float bf2f(u16 v){ return __uint_as_float(((unsigned)v)<<16); }
__device__ __forceinline__ u16 f2bf(float f){ __hip_bfloat16 h = __float2bfloat16(f); return *(u16*)&h; }

__global__ __launch_bounds__(256) void gemm64(const u16* A, const u16* B, u16* C, int M, int N, int K, int lda, int ldb, int ldc) {
  __shared__ u16 As[64][40];
  __shared__ u16 Bt[64][40];
  int tid = threadIdx.x, wid = tid>>6, lane = tid&63;
  int bm = blockIdx.y*64, bn = blockIdx.x*64;
  int wr = wid>>1, wc = wid&1;
  f32x4 acc[2][2] = {};
  for (int k0=0;k0<K;k0+=32) {
    { int r = tid>>2, c = (tid&3)*8; *(uint4*)&As[r][c] = *(const uint4*)&A[(size_t)(bm+r)*lda + k0 + c]; }
    { int r = tid>>3, c = (tid&7)*8; uint4 v = *(const uint4*)&B[(size_t)(k0+r)*ldb + bn + c]; u16* p=(u16*)&v;
      for (int j=0;j<8;++j) Bt[c+j][r] = p[j]; }
    __syncthreads();
    bf16x8 a[2], b[2];
    for (int i=0;i<2;++i){ a[i] = *(bf16x8*)&As[wr*32+i*16+(lane&15)][(lane>>4)*8]; b[i] = *(bf16x8*)&Bt[wc*32+i*16+(lane&15)][(lane>>4)*8]; }
    for (int i=0;i<2;++i) for (int j=0;j<2;++j) acc[i][j] = __builtin_amdgcn_mfma_f32_16x16x32_bf16(a[i], b[j], acc[i][j],0,0,0);
    __syncthreads();
  }
  for (int i=0;i<2;++i) for (int j=0;j<2;++j) for (int r=0;r<4;++r)
    C[(size_t)(bm+wr*32+i*16+(lane>>4)*4+r)*ldc + bn+wc*32+j*16+(lane&15)] = f2bf(acc[i][j][r]);
}

__global__ __launch_bounds__(256) void feat(const u16* qkv, const u16* wphi, float* Fq, float* Fk, int isB) {
  int idx = blockIdx.x*blockDim.x+threadIdx.x;
  int which = idx / (2048*32); int r = idx % (2048*32); int t = r/32, h = r%32;
  const u16* src = qkv + (size_t)t*1728 + which*576 + h*18;
  float* dst = (which? Fk : Fq) + (size_t)r*36;
  float x[18];
  for (int d=0;d<18;++d) x[d] = bf2f(src[d]);
  if (!isB) {
    for (int d=0;d<18;++d) { float v = x[d]>0 ? x[d]+1.f : __expf(x[d]); dst[d] = v + 1e-6f; dst[18+d]=0.f; }
  } else {
    float p[18]; float mx=-1e30f, mn=1e30f;
    for (int f=0;f<18;++f){ float s=0; for (int d=0;d<18;++d) s += x[d]*bf2f(wphi[(h*18+d)*18+f]); p[f]=s; mx=fmaxf(mx,s); mn=fminf(mn,s);}
    float sp=0, sn=0; float e1[18], e2[18];
    for (int f=0;f<18;++f){ e1[f]=__expf(p[f]-mx); e2[f]=__expf(mn-p[f]); sp+=e1[f]; sn+=e2[f]; }
    for (int f=0;f<18;++f){ dst[f]=e1[f]/sp; dst[18+f]=e2[f]/sn; }
  }
}
__global__ __launch_bounds__(256) void kvk(const float* Fk, const u16* qkv, float* kv) {
  int h = blockIdx.x/36, f = blockIdx.x%36; __shared__ float red[256][19];
  float s[19]; for (int e=0;e<19;++e) s[e]=0;
  for (int t=threadIdx.x;t<2048;t+=256){ float p = Fk[((size_t)t*32+h)*36+f]; const u16* v = qkv+(size_t)t*1728+1152+h*18;
    for (int e=0;e<18;++e) s[e]+=p*bf2f(v[e]); s[18]+=p; }
  for (int e=0;e<19;++e) red[threadIdx.x][e]=s[e];
  __syncthreads();
  if (threadIdx.x<19){ float a=0; for (int i=0;i<256;++i) a+=red[i][threadIdx.x]; kv[(h*36+f)*19+threadIdx.x]=a; }
}
__global__ __launch_bounds__(256) void ok(const float* Fq, const float* kv, u16* o) {
  int r = blockIdx.x*blockDim.x+threadIdx.x; int t=r/32, h=r%32;
  float q[36]; for (int f=0;f<36;++f) q[f]=Fq[(size_t)r*36+f];
  float den=1e-6f; for (int f=0;f<36;++f) den += q[f]*kv[(h*36+f)*19+18];
  for (int e=0;e<18;++e){ float n=0; for (int f=0;f<36;++f) n+=q[f]*kv[(h*36+f)*19+e]; o[(size_t)t*576+h*18+e]=f2bf(n/den); }
}
extern "C" void kernel_launch(void* const* d_in, const int* in_sizes, int n_in,
                              void* d_out, int out_size, void* d_ws, size_t ws_size,
                              hipStream_t stream) {
  char* ws = (char*)d_ws;
  u16* qkv = (u16*)ws; float* Fq = (float*)(ws + (8<<20)); float* Fk = (float*)(ws + (24<<20));
  float* kv = (float*)(ws + (40<<20)); u16* o = (u16*)(ws + (41<<20));
  u16* out = (u16*)d_out;
  for (int p=0;p<2;++p){
    const u16* x = (const u16*)d_in[p?3:0]; const u16* w = (const u16*)d_in[p?4:1]; const u16* wo = (const u16*)d_in[p?6:2];
    gemm64<<<dim3(27,32),256,0,stream>>>(x,w,qkv,2048,1728,576,576,1728,1728);
    feat<<<2048*32*2/256,256,0,stream>>>(qkv,(const u16*)d_in[5],Fq,Fk,p);
    kvk<<<32*36,256,0,stream>>>(Fk,qkv,kv);
    ok<<<2048*32/256,256,0,stream>>>(Fq,kv,o);
    gemm64<<<dim3(9,32),256,0,stream>>>(o,wo,out+p*576,2048,576,576,576,576,1152);
  }
}
```

```cpp
#include <hip/hip_runtime.h>
#include <stdint.h>
#include <utility>
#include <type_traits>

typedef __bf16 bf16x2 __attribute__((ext_vector_type(2)));
typedef __bf16 bf16x4 __attribute__((ext_vector_type(4)));
typedef __bf16 bf16x8 __attribute__((ext_vector_type(8)));
typedef float f32x2 __attribute__((ext_vector_type(2)));
typedef float f32x4 __attribute__((ext_vector_type(4)));
typedef float f32x16 __attribute__((ext_vector_type(16)));
typedef unsigned u32x2 __attribute__((ext_vector_type(2)));
typedef unsigned u32x4 __attribute__((ext_vector_type(4)));
typedef unsigned long long u64;
#define LAS __attribute__((address_space(3)))
#define GAS __attribute__((address_space(1)))
#define DI __device__ __forceinline__
#define GAP() __builtin_amdgcn_sched_barrier(0)
#define RLX_AGENT __ATOMIC_RELAXED, __HIP_MEMORY_SCOPE_AGENT

constexpr int BLK = 4608;
constexpr int SLOT = 8 * BLK;
constexpr int L_WQ = 8 * BLK;
constexpr int L_IMG2 = 26 * BLK;
constexpr int L_WPHI = 4 * SLOT;
constexpr int L_DUMMY = 150528;
constexpr int L_ZERO = 150784;
constexpr int L_PW = 151552;
constexpr int LDS_BYTES = 163840;
constexpr int PITCH_A = 288, PITCH_B = 480;
constexpr int KVP_A = 20, KVP_B = 36;
constexpr int KVN_A = 19 * KVP_A, KVN_B = 19 * KVP_B;

constexpr size_t W_EP = 0;
constexpr size_t W_INIT = 1024;
constexpr size_t W_KVF = 4096;
constexpr size_t W_OF = 8192;
constexpr size_t W_KV = 16384;
constexpr size_t KVG = 11264;
constexpr size_t W_O = 262144;
constexpr size_t W_STAMP = 8u << 20;
constexpr unsigned SPIN_CAP = 1u << 17;

#define STAMP(i) do { } while (0)

DI void dma16(unsigned voff, const char* sbase, unsigned ldsaddr) {
  asm volatile("s_mov_b32 m0, %2\n\ts_nop 0\n\tglobal_load_lds_dwordx4 %0, %1" :: "v"(voff), "s"(sbase), "s"(ldsaddr) : "memory");
}
DI void dma16nt(unsigned voff, const char* sbase, unsigned ldsaddr) {
  asm volatile("s_mov_b32 m0, %2\n\ts_nop 0\n\tglobal_load_lds_dwordx4 %0, %1 nt" :: "v"(voff), "s"(sbase), "s"(ldsaddr) : "memory");
}
DI void dma16_lo(unsigned voff, const char* sbase, unsigned ldsaddr) {
  asm volatile("s_mov_b32 m0, %2\n\ts_mov_b32 exec_hi, 0\n\tglobal_load_lds_dwordx4 %0, %1\n\ts_mov_b32 exec_hi, -1" :: "v"(voff), "s"(sbase), "s"(ldsaddr) : "memory");
}
DI void dma16_q(unsigned voff, const char* sbase, unsigned ldsaddr) {
  asm volatile("s_mov_b32 m0, %2\n\ts_mov_b64 exec, 0xffff\n\tglobal_load_lds_dwordx4 %0, %1\n\ts_mov_b64 exec, -1" :: "v"(voff), "s"(sbase), "s"(ldsaddr) : "memory");
}
DI bf16x8 trfrag(LAS char* p0, LAS char* p1) {
  bf16x4 a = __builtin_amdgcn_ds_read_tr16_b64_v4bf16((LAS bf16x4*)p0);
  bf16x4 b = __builtin_amdgcn_ds_read_tr16_b64_v4bf16((LAS bf16x4*)p1);
  return __builtin_shufflevector(a, b, 0, 1, 2, 3, 4, 5, 6, 7);
}
DI void mfma_va(f32x4& d, const bf16x8& a, const bf16x8& b) {
  asm volatile("v_mfma_f32_16x16x32_bf16 %0, %1, %2, %0" : "+v"(d) : "v"(a), "a"(b));
}
DI void mfma_vv(f32x4& d, const bf16x8& a, const bf16x8& b) {
  asm volatile("v_mfma_f32_16x16x32_bf16 %0, %1, %2, %0" : "+v"(d) : "v"(a), "v"(b));
}
DI void mfma_vn(f32x4& d, const bf16x8& a, const bf16x8& b) {
  asm volatile("s_nop 1\n\tv_mfma_f32_16x16x32_bf16 %0, %1, %2, %0" : "+v"(d) : "v"(a), "v"(b));
}
DI void mfma_vv0(f32x4& d, const bf16x8& a, const bf16x8& b) {
  asm volatile("s_nop 1\n\tv_mfma_f32_16x16x32_bf16 %0, %1, %2, 0" : "=&v"(d) : "v"(a), "v"(b));
}
DI void mfma32(f32x16& d, const bf16x8& a, const bf16x8& b) {
  asm volatile("s_nop 1\n\tv_mfma_f32_32x32x16_bf16 %0, %1, %2, %0" : "+v"(d) : "v"(a), "v"(b));
}
DI void mfma32_0(f32x16& d, const bf16x8& a, const bf16x8& b) {
  asm volatile("s_nop 1\n\tv_mfma_f32_32x32x16_bf16 %0, %1, %2, 0" : "=&v"(d) : "v"(a), "v"(b));
}
template <int... I, class F> DI void sfor_(std::integer_sequence<int, I...>, F&& f) { (f(std::integral_constant<int, I>{}), ...); }
template <int N, class F> DI void sfor(F&& f) { sfor_(std::make_integer_sequence<int, N>{}, f); }
#define WAITV(n) asm volatile("s_waitcnt vmcnt(" #n ")" ::: "memory")
#define WAITL0() asm volatile("s_waitcnt lgkmcnt(0)" ::: "memory")
#define BAR() asm volatile("s_barrier" ::: "memory")
#define LBAR() asm volatile("s_waitcnt lgkmcnt(0)\n\ts_barrier" ::: "memory")

DI unsigned cvtpk(float lo, float hi) { f32x2 v = {lo, hi}; bf16x2 b = __builtin_convertvector(v, bf16x2); return __builtin_bit_cast(unsigned, b); }
DI u32x2 pack4(const f32x4& x) { return u32x2{cvtpk(x[0], x[1]), cvtpk(x[2], x[3])}; }
DI bf16x8 swap_pair(const u32x2& a, const u32x2& b) {
  auto r0 = __builtin_amdgcn_permlane16_swap(a[0], b[0], false, false);
  auto r1 = __builtin_amdgcn_permlane16_swap(a[1], b[1], false, false);
  u32x4 t = {r0[0], r1[0], r0[1], r1[1]};
  return __builtin_bit_cast(bf16x8, t);
}
DI float phiA(float t) {
  const float L2E = 1.4426950408889634f;
  float e = __builtin_amdgcn_exp2f(__builtin_fmaf(t, L2E, -1e-6f * L2E));
  e = __builtin_amdgcn_fmed3f(e, 0.f, 1.f);
  return __builtin_fmaxf(t, 1e-6f) + e;
}
DI bf16x8 ldfrag8(LAS char* p) {
  f32x4 a = *(LAS f32x4*)p, b = *(LAS f32x4*)(p + 16);
  u32x4 t = {cvtpk(a[0], a[1]), cvtpk(a[2], a[3]), cvtpk(b[0], b[1]), cvtpk(b[2], b[3])};
  return __builtin_bit_cast(bf16x8, t);
}
DI void st16_sc1(char* p, const u32x4& v) {
  asm volatile("global_store_dwordx4 %0, %1, off sc1\n\ts_nop 1" :: "v"(p), "v"(v) : "memory");
}
DI void st8_sc1(char* p, const u32x2& v) {
  asm volatile("global_store_dwordx2 %0, %1, off sc1" :: "v"(p), "v"(v) : "memory");
}

DI void hedgehog(LAS char* pw, int lane, const bf16x8& b0, const bf16x8& b1, const bf16x8& br, unsigned (&pp)[8], unsigned (&pm)[8], unsigned& rp, unsigned& rm) {
  LAS char* q = pw + lane * 16;
  bf16x8 w0 = *(LAS bf16x8*)(q), w1 = *(LAS bf16x8*)(q + 1024), w2 = *(LAS bf16x8*)(q + 2048);
  bf16x8 w3 = *(LAS bf16x8*)(q + 3072), w4 = *(LAS bf16x8*)(q + 4096), w5 = *(LAS bf16x8*)(q + 5120);
  f32x16 D, D2;
  mfma32_0(D, w0, b0); mfma32_0(D2, w3, b0);
  mfma32(D, w1, b1); mfma32(D2, w4, b1);
  mfma32(D, w2, br); mfma32(D2, w5, br);
  asm volatile("s_nop 7\n\ts_nop 3" : "+v"(D), "+v"(D2));
  float x[18];
#pragma unroll
  for (int i = 0; i < 16; ++i) x[i] = D[i];
  x[16] = D2[0]; x[17] = D2[1];
  float M = x[0], m = x[0];
#pragma unroll
  for (int i = 1; i < 18; ++i) { M = __builtin_fmaxf(M, x[i]); m = __builtin_fminf(m, x[i]); }
  const float L2E = 1.4426950408889634f;
  const float ap = -M * L2E, am = m * L2E;
  float ep[18], em[18], sp = 0.f, sm = 0.f;
#pragma unroll
  for (int i = 0; i < 18; ++i) {
    ep[i] = __builtin_amdgcn_exp2f(__builtin_fmaf(x[i], L2E, ap));
    em[i] = __builtin_amdgcn_exp2f(__builtin_fmaf(x[i], -L2E, am));
    sp += ep[i]; sm += em[i];
  }
  const float rpn = __builtin_amdgcn_rcpf(sp), rmn = __builtin_amdgcn_rcpf(sm);
#pragma unroll
  for (int i = 0; i < 8; ++i) { pp[i] = cvtpk(ep[2 * i] * rpn, ep[2 * i + 1] * rpn); pm[i] = cvtpk(em[2 * i] * rmn, em[2 * i + 1] * rmn); }
  rp = cvtpk(ep[16] * rpn, ep[17] * rpn); rm = cvtpk(em[16] * rmn, em[17] * rmn);
}

DI bool wait_flags(const u64* f, int n, u64 tag, int lane) {
  const GAS u64* p = (const GAS u64*)f + (lane < n ? lane : 0);
#pragma nounroll
  for (unsigned spins = 0; spins < SPIN_CAP; ++spins) {
    u64 v = __hip_atomic_load(p, RLX_AGENT);
    if (__all(v == tag)) return true;
    __builtin_amdgcn_s_sleep(1);
  }
  return false;
}

__global__ __launch_bounds__(256, 1) void fused_kernel(const char* __restrict__ xa, const char* __restrict__ wa, const char* __restrict__ woa,
                                                       const char* __restrict__ xb, const char* __restrict__ wb, const char* __restrict__ wphi,
                                                       const char* __restrict__ wob, char* __restrict__ out, char* __restrict__ ws) {
  extern __shared__ __attribute__((aligned(16))) char lds_[];
  LAS char* lds = (LAS char*)lds_;
  const unsigned ldsb = (unsigned)(size_t)lds_;
  const int tid = threadIdx.x, lane = tid & 63;
  const int w = __builtin_amdgcn_readfirstlane(tid >> 6);
  const int b = blockIdx.x, xcd = b & 7, r = b >> 3;
  const int pb = xcd >> 2;
  const int tt = (xcd & 3) * 4 + (r >> 3), hg = r & 7;
  const char* X = (pb ? xb : xa) + (size_t)(tt * 128 + 32 * w) * 1152;
  const char* W = (pb ? wb : wa) + hg * 144;
  const char* Wo = (pb ? wob : woa) + hg * 144;
  const int grp = pb * 8 + hg;
  STAMP(0);
  const int g = lane >> 4, q4 = (lane & 15) >> 2, p4 = lane & 3, t16 = lane & 15;
  const int t32 = lane & 31, hh = lane >> 5;

  u64 ep;
  asm volatile("global_load_dwordx2 %0, %1, off sc1" : "=v"(ep) : "v"(ws + W_EP) : "memory");
  char* kvg = ws + W_KV + (size_t)grp * KVG;
  if (tt == 0) {
    const u32x4 z = {0, 0, 0, 0};
#pragma unroll
    for (int i = 0; i < 3; ++i) { int c = tid + 256 * i; if (c < (int)(KVG / 16)) st16_sc1(kvg + c * 16, z); }
  }
  { int c = 64 * w + lane; if (c > 161) c = 161; dma16(c * 16, wphi + hg * 2592, ldsb + L_WPHI + w * 1024); }

  unsigned xoff[4], woff[5];
#pragma unroll
  for (int i = 0; i < 4; ++i) { int row = 8 * i + (lane >> 3); int f = ((row & 15) >> 1) & 7; xoff[i] = row * 1152 + (((lane & 7) ^ f) << 4); }
#pragma unroll
  for (int i = 0; i < 5; ++i) { int c = 64 * i + lane; if (c > 287) c = 287; int row = c / 18, cc = c % 18;
    int k = 32 * (w >> 1) + 8 * (row >> 2) + 4 * (w & 1) + (row & 3); woff[i] = k * 3456 + (1 + cc / 9) * 1152 + (cc % 9) * 16; }
  auto issue_kv = [&](int c) {
    const unsigned slot = ldsb + (c & 3) * SLOT;
#pragma unroll
    for (int i = 0; i < 4; ++i) dma16nt(xoff[i], X + c * 128, slot + (4 + w) * BLK + i * 1024);
#pragma unroll
    for (int i = 0; i < 4; ++i) dma16(woff[i], W + (size_t)c * 64 * 3456, slot + w * BLK + i * 1024);
    dma16_lo(woff[4], W + (size_t)c * 64 * 3456, slot + w * BLK + 4096);
  };
  issue_kv(0); issue_kv(1); issue_kv(2);

  unsigned qoff[5];
#pragma unroll
  for (int i = 0; i < 5; ++i) { int c = 64 * i + lane; if (c > 287) c = 287; int row = c / 18, cc = c % 18;
    int k = 8 * (row >> 2) + 4 * (cc / 9) + (row & 3); qoff[i] = k * 3456 + (cc % 9) * 16; }
  auto issue_q = [&](int s) {
    const unsigned dst = ldsb + L_WQ + s * BLK; const char* src = W + (size_t)s * 32 * 3456;
#pragma unroll
    for (int i = 0; i < 4; ++i) dma16(qoff[i], src, dst + i * 1024);
    dma16_lo(qoff[4], src, dst + 4096);
  };

  const unsigned Lkv = (4 * g + q4) * 288 + 8 * p4, Lrem = (4 * g + q4) * 288 + 32 + 72 * p4;
  const unsigned Lx0 = (4 + w) * BLK + t16 * 128 + ((g ^ ((t16 >> 1) & 7)) << 4), Lx1 = Lx0 ^ 64;
  const float ini = pb ? 0.f : 1e-6f, inir = (pb == 0 && g < 2) ? 1e-6f : 0.f;
  f32x4 acc[9][2];
#pragma unroll
  for (int i = 0; i < 9; ++i) { const float v = i < 4 ? ini : (i < 8 ? 0.f : inir); acc[i][0] = f32x4{v, v, v, v}; acc[i][1] = f32x4{v, v, v, v}; }
  if (tid < 64) *(LAS unsigned*)(lds + L_ZERO + tid * 4) = 0u;
  bf16x8 xf[2][18];
  u64 tag = 0, initv;

#pragma unroll
  for (int c = 0; c < 9; ++c) {
    if (c == 0) { asm volatile("s_waitcnt vmcnt(18)" : "+v"(ep) :: "memory"); }
    else if (c <= 6) WAITV(18);
    else if (c == 7) WAITV(19);
    else asm volatile("s_waitcnt vmcnt(20)" : "+v"(initv) :: "memory");
    LBAR();
    if (c == 0) {
      tag = ((u64)__builtin_amdgcn_readfirstlane((unsigned)(ep >> 32)) << 32 | (u64)__builtin_amdgcn_readfirstlane((unsigned)ep)) + 1;
      if (tt == 0 && tid == 0) __hip_atomic_store((GAS u64*)(ws + W_INIT + grp * 128), tag, RLX_AGENT);
    }
    LAS char* slot = lds + (c & 3) * SLOT;
#pragma unroll
    for (int s = 0; s < 2; ++s)
#pragma unroll
      for (int a = 0; a < 2; ++a) xf[a][2 * c + s] = *(LAS bf16x8*)(slot + (s ? Lx1 : Lx0) + a * 2048);
    auto rd = [&](int n) { const int s = n / 9, tl = n % 9; constexpr int moff[4] = {0, 40, 72, 112};
      LAS char* pa = slot + (2 * s) * BLK + (tl < 8 ? Lkv + (tl >> 2) * 144 + moff[tl & 3] : Lrem);
      return trfrag(pa, pa + BLK); };
    bf16x8 wf[18];
    wf[0] = rd(0); wf[1] = rd(1);
    GAP();
    if (c + 3 <= 8) issue_kv(c + 3);
    if (c == 5) asm volatile("global_load_dwordx2 %0, %1, off sc1" : "=v"(initv) : "v"(ws + W_INIT + grp * 128) : "memory");
    if (c == 6) { issue_q(w); issue_q(4 + w); }
    if (c == 7) { issue_q(8 + w); issue_q(12 + w); }
    if (c == 8) {
      const int s = 16 + (w >> 1), c0 = 144 * (w & 1);
      const unsigned dst = ldsb + L_WQ + s * BLK + c0 * 16; const char* src = W + (size_t)s * 32 * 3456;
#pragma unroll
      for (int i = 0; i < 3; ++i) { int cidx = c0 + 64 * i + lane; if (cidx > 287) cidx = 287; int row = cidx / 18, cc = cidx % 18;
        unsigned o = (8 * (row >> 2) + 4 * (cc / 9) + (row & 3)) * 3456 + (cc % 9) * 16;
        if (i < 2) dma16(o, src, dst + i * 1024); else dma16_q(o, src, dst + 2048); }
    }
    if (c == 0 && pb) {
#pragma unroll
      for (int i = 0; i < 3; ++i) {
        const int f = 3 * w + i, P = f / 6, kind = f % 6, k3 = kind % 3;
        const int rho = t32, hrow = (rho >> 2) & 1;
        const bool d2 = kind >= 3;
        const int fo = d2 ? 16 + (rho & 3) : (rho & 3) + 4 * (rho >> 3);
        const bool valid = d2 ? (rho < 8 && (rho & 3) < 2) : true;
        unsigned short v[8];
#pragma unroll
        for (int j = 0; j < 8; ++j) {
          int d; bool nz;
          if (k3 < 2) { d = 8 * hh + j + 2 * k3; nz = valid && hrow == k3; }
          else { d = hrow ? (j & 1) : 16 + (j & 1); nz = valid && hh == 0 && (j >> 2) == P && ((j >> 1) & 1) == hrow; }
          const int head = 2 * P + (k3 < 2 ? k3 : hrow);
          const unsigned short x = *(LAS unsigned short*)(lds + L_WPHI + ((head * 18 + d) * 18 + (valid ? fo : 0)) * 2);
          v[j] = nz ? x : (unsigned short)0;
        }
        u32x4 t = {v[0] | (unsigned)v[1] << 16, v[2] | (unsigned)v[3] << 16, v[4] | (unsigned)v[5] << 16, v[6] | (unsigned)v[7] << 16};
        *(LAS u32x4*)(lds + L_PW + f * 1024 + lane * 16) = t;
      }
    }
    GAP();
#pragma unroll
    for (int n = 0; n < 18; ++n) {
      if (n + 2 < 18) wf[n + 2] = rd(n + 2);
      GAP();
      mfma_va(acc[n % 9][0], wf[n], xf[0][2 * c + n / 9]);
      mfma_va(acc[n % 9][1], wf[n], xf[1][2 * c + n / 9]);
      GAP();
    }
  }
  asm volatile("s_nop 7" : "+v"(acc[0][0]), "+v"(acc[0][1]), "+v"(acc[1][0]), "+v"(acc[1][1]), "+v"(acc[2][0]), "+v"(acc[2][1]), "+v"(acc[3][0]), "+v"(acc[3][1]), "+v"(acc[4][0]),
                           "+v"(acc[4][1]), "+v"(acc[5][0]), "+v"(acc[5][1]), "+v"(acc[6][0]), "+v"(acc[6][1]), "+v"(acc[7][0]), "+v"(acc[7][1]), "+v"(acc[8][0]), "+v"(acc[8][1]));
  LBAR();
  STAMP(1);

  const int sw16 = (t16 >> 2) & 3;
  const unsigned trp = (p4 ^ g) << 3;
  f32x4 kt[9];
  if (pb == 0) {
    LAS char* row0 = lds + (32 * w + t16) * PITCH_A + ((g ^ sw16) << 3);
#pragma unroll
    for (int a = 0; a < 2; ++a) {
      LAS char* rp_ = row0 + a * 16 * PITCH_A;
#pragma unroll
      for (int h = 0; h < 4; ++h) {
        f32x4 k = acc[h][a];
        k = f32x4{phiA(k[0]), phiA(k[1]), phiA(k[2]), phiA(k[3])};
        *(LAS u32x2*)(rp_ + h * 32) = pack4(k);
        *(LAS u32x2*)(rp_ + 128 + h * 32) = pack4(acc[4 + h][a]);
      }
      f32x4 rr = acc[8][a];
      f32x4 ph = f32x4{phiA(rr[0]), phiA(rr[1]), phiA(rr[2]), phiA(rr[3])};
      if (g < 2) rr = ph;
      *(LAS u32x2*)(rp_ + 256) = pack4(rr);
    }
    LBAR();
    const bf16x8 ones = __builtin_bit_cast(bf16x8, (u32x4{0x3f803f80u, 0x3f803f80u, 0x3f803f80u, 0x3f803f80u}));
#pragma unroll
    for (int u = 0; u < 4; ++u) {
      LAS char* base = lds + (32 * u + 4 * g + q4) * PITCH_A + trp;
      bf16x8 F0 = trfrag(base + w * 32, base + w * 32 + 16 * PITCH_A);
      bf16x8 E0 = trfrag(base + 128 + w * 32, base + 128 + w * 32 + 16 * PITCH_A);
      bf16x8 R = trfrag(base + 256, base + 256 + 16 * PITCH_A);
      if (u == 0) { mfma_vv0(kt[0], F0, E0); mfma_vv0(kt[1], F0, R); mfma_vv0(kt[2], R, E0); mfma_vv0(kt[3], R, R); mfma_vv0(kt[4], F0, ones); mfma_vv0(kt[5], R, ones); }
      else { mfma_vn(kt[0], F0, E0); mfma_vn(kt[1], F0, R); mfma_vn(kt[2], R, E0); mfma_vn(kt[3], R, R); mfma_vn(kt[4], F0, ones); mfma_vn(kt[5], R, ones); }
    }
    asm volatile("s_nop 7" : "+v"(kt[0]), "+v"(kt[1]), "+v"(kt[2]), "+v"(kt[3]), "+v"(kt[4]), "+v"(kt[5]));
    LAS char* cp = lds + L_IMG2 + w * 1536;
    const int c0 = 8 + 4 * (w >> 1) + 2 * (w & 1), cz = (c0 + 2) & 15;
    *(LAS f32x4*)(cp + (t16 * KVP_A + 4 * g) * 4) = kt[0];
    const bool sel = (t16 == c0) | (t16 == c0 + 1) | (t16 == cz);
    const int erow = t16 == cz ? 18 : 16 + (t16 - c0);
    if (sel) *(LAS f32x4*)(cp + (erow * KVP_A + 4 * g) * 4) = (t16 == cz) ? kt[4] : kt[1];
    const bool odd = w & 1;
    if (g == (w >> 1)) {
      *(LAS f32x2*)(cp + (t16 * KVP_A + 16) * 4) = odd ? f32x2{kt[2][2], kt[2][3]} : f32x2{kt[2][0], kt[2][1]};
      if (sel) { const f32x4 v = (t16 == cz) ? kt[5] : kt[3]; *(LAS f32x2*)(cp + (erow * KVP_A + 16) * 4) = odd ? f32x2{v[2], v[3]} : f32x2{v[0], v[1]}; }
    }
    WAITL0();
    if (__builtin_amdgcn_readfirstlane((unsigned)initv) != (unsigned)tag || __builtin_amdgcn_readfirstlane((unsigned)(initv >> 32)) != (unsigned)(tag >> 32))
      wait_flags((const u64*)(ws + W_INIT + grp * 128), 1, tag, lane);
    GAS float* dst = (GAS float*)(kvg + w * KVN_A * 4);
#pragma unroll
    for (int i = 0; i < 6; ++i) { const int idx = 64 * i + lane; const int e = idx / KVP_A, f = idx % KVP_A;
      if (idx < KVN_A && f < 18) { const float v = *(LAS float*)(cp + idx * 4); __hip_atomic_fetch_add(dst + idx, v, RLX_AGENT); } }
    WAITV(6);
  } else {
    const bf16x8 Rb = swap_pair(pack4(acc[8][0]), pack4(acc[8][1]));
    LAS char* img = lds + (w < 2 ? 0 : L_IMG2 - 64 * PITCH_B);
    const int sw32 = (t32 >> 2) & 3;
    LAS char* row32 = img + (32 * w + t32) * PITCH_B;
#pragma unroll
    for (int P = 0; P < 2; ++P) {
      const bf16x8 b0 = swap_pair(pack4(acc[2 * P][0]), pack4(acc[2 * P][1])), b1 = swap_pair(pack4(acc[2 * P + 1][0]), pack4(acc[2 * P + 1][1]));
      unsigned pp[8], pm[8], rp, rm;
      hedgehog(lds + L_PW + P * 6144, lane, b0, b1, Rb, pp, pm, rp, rm);
      LAS char* d = row32 + (2 * P + hh) * 32;
#pragma unroll
      for (int pc = 0; pc < 4; ++pc) {
        *(LAS u32x2*)(d + ((pc ^ sw32) << 3)) = u32x2{pp[2 * pc], pp[2 * pc + 1]};
        *(LAS u32x2*)(d + 128 + ((pc ^ sw32) << 3)) = u32x2{pm[2 * pc], pm[2 * pc + 1]};
      }
      *(LAS u32x2*)(row32 + 384 + (((2 * P + hh) ^ sw32) << 3)) = u32x2{rp, rm};
    }
#pragma unroll
    for (int a = 0; a < 2; ++a) {
      LAS char* rp_ = img + (32 * w + 16 * a + t16) * PITCH_B;
#pragma unroll
      for (int h = 0; h < 4; ++h) *(LAS u32x2*)(rp_ + 256 + h * 32 + ((g ^ sw16) << 3)) = pack4(acc[4 + h][a]);
      if (g >= 2) *(LAS u32x2*)(rp_ + 416 + (((g - 2) ^ sw16) << 3)) = pack4(acc[8][a]);
    }
    LBAR();
    const bf16x8 ones = __builtin_bit_cast(bf16x8, (u32x4{0x3f803f80u, 0x3f803f80u, 0x3f803f80u, 0x3f803f80u}));
#pragma unroll
    for (int u = 0; u < 4; ++u) {
      LAS char* base = lds + (u < 2 ? 0 : L_IMG2 - 64 * PITCH_B) + (32 * u + 4 * g + q4) * PITCH_B + trp;
      constexpr int H2 = 16 * PITCH_B;
      bf16x8 Fp = trfrag(base + w * 32, base + w * 32 + H2), Fm = trfrag(base + 128 + w * 32, base + 128 + w * 32 + H2);
      bf16x8 E0 = trfrag(base + 256 + w * 32, base + 256 + w * 32 + H2);
      bf16x8 Fr = trfrag(base + 384, base + 384 + H2), E1 = trfrag(base + 416, base + 416 + H2);
      if (u == 0) { mfma_vv0(kt[0], Fp, E0); mfma_vv0(kt[1], Fm, E0); mfma_vv0(kt[2], Fr, E0); mfma_vv0(kt[3], Fp, E1); mfma_vv0(kt[4], Fm, E1); mfma_vv0(kt[5], Fr, E1);
                    mfma_vv0(kt[6], Fp, ones); mfma_vv0(kt[7], Fm, ones); mfma_vv0(kt[8], Fr, ones); }
      else { mfma_vn(kt[0], Fp, E0); mfma_vn(kt[1], Fm, E0); mfma_vn(kt[2], Fr, E0); mfma_vn(kt[3], Fp, E1); mfma_vn(kt[4], Fm, E1); mfma_vn(kt[5], Fr, E1);
             mfma_vn(kt[6], Fp, ones); mfma_vn(kt[7], Fm, ones); mfma_vn(kt[8], Fr, ones); }
    }
    asm volatile("s_nop 7" : "+v"(kt[0]), "+v"(kt[1]), "+v"(kt[2]), "+v"(kt[3]), "+v"(kt[4]), "+v"(kt[5]), "+v"(kt[6]), "+v"(kt[7]), "+v"(kt[8]));
    LBAR();
    LAS char* cp = lds + w * (KVN_B * 4);
    const int c0 = 4 * (w >> 1) + 2 * (w & 1), cz = (c0 + 2) & 15;
    const bool sel = (t16 == c0) | (t16 == c0 + 1) | (t16 == cz);
    const int erow = t16 == cz ? 18 : 16 + (t16 - c0);
    *(LAS f32x4*)(cp + (t16 * KVP_B + 4 * g) * 4) = kt[0];
    *(LAS f32x4*)(cp + (t16 * KVP_B + 16 + 4 * g) * 4) = kt[1];
    if (g == w) *(LAS f32x4*)(cp + (t16 * KVP_B + 32) * 4) = kt[2];
    if (sel) {
      const bool z = t16 == cz;
      *(LAS f32x4*)(cp + (erow * KVP_B + 4 * g) * 4) = z ? kt[6] : kt[3];
      *(LAS f32x4*)(cp + (erow * KVP_B + 16 + 4 * g) * 4) = z ? kt[7] : kt[4];
      if (g == w) *(LAS f32x4*)(cp + (erow * KVP_B + 32) * 4) = z ? kt[8] : kt[5];
    }
    WAITL0();
    if (__builtin_amdgcn_readfirstlane((unsigned)initv) != (unsigned)tag || __builtin_amdgcn_readfirstlane((unsigned)(initv >> 32)) != (unsigned)(tag >> 32))
      wait_flags((const u64*)(ws + W_INIT + grp * 128), 1, tag, lane);
    GAS float* dst = (GAS float*)(kvg + w * KVN_B * 4);
#pragma unroll
    for (int i = 0; i < 11; ++i) { const int idx = 64 * i + lane;
      if (idx < KVN_B) { const float v = *(LAS float*)(cp + idx * 4); __hip_atomic_fetch_add(dst + idx, v, RLX_AGENT); } }
    WAITV(11);
  }
  LBAR();
  STAMP(2);

  const unsigned Lq = L_WQ + (4 * g + q4) * 288 + 8 * p4, Lqr = L_WQ + (4 * g + q4) * 288 + 32 + 72 * (p4 & 1);
  f32x4 qa[5][2];
#pragma unroll
  for (int i = 0; i < 5; ++i) { qa[i][0] = f32x4{ini, ini, ini, ini}; qa[i][1] = f32x4{ini, ini, ini, ini}; }
  {
    auto rd = [&](int n) { const int s = n / 5, tl = n % 5; constexpr int moff[4] = {0, 40, 72, 112};
      LAS char* pa = lds + s * BLK + (tl < 4 ? Lq + moff[tl] : Lqr);
      return trfrag(pa, pa + 144); };
    bf16x8 wf[90];
    wf[0] = rd(0); wf[1] = rd(1);
    asm volatile("s_nop 1" : "+v"(qa[0][0]), "+v"(qa[0][1]), "+v"(qa[1][0]), "+v"(qa[1][1]), "+v"(qa[2][0]), "+v"(qa[2][1]), "+v"(qa[3][0]), "+v"(qa[3][1]), "+v"(qa[4][0]), "+v"(qa[4][1]));
#pragma unroll
    for (int n = 0; n < 90; ++n) {
      if (n + 2 < 90) wf[n + 2] = rd(n + 2);
      GAP();
      mfma_va(qa[n % 5][0], wf[n], xf[0][n / 5]);
      mfma_va(qa[n % 5][1], wf[n], xf[1][n / 5]);
      GAP();
    }
  }
  asm volatile("s_nop 7" : "+v"(qa[0][0]), "+v"(qa[0][1]), "+v"(qa[1][0]), "+v"(qa[1][1]), "+v"(qa[2][0]), "+v"(qa[2][1]), "+v"(qa[3][0]), "+v"(qa[3][1]), "+v"(qa[4][0]), "+v"(qa[4][1]));
  WAITV(0);
  LBAR();
  if (tid == 0) __hip_atomic_store((GAS u64*)(ws + W_KVF + grp * 128 + tt * 8), tag, RLX_AGENT);
  STAMP(3);
  {
    unsigned o1[5], o2[5];
#pragma unroll
    for (int i = 0; i < 5; ++i) { int c = 64 * i + lane; if (c > 287) c = 287; int row = c / 18, cc = c % 18;
      const int gg = row >> 2, j = 4 * (cc / 9) + (row & 3);
      o1[i] = (20 * (gg >> 1) + 8 * (gg & 1) + j) * 1152 + (cc % 9) * 16;
      o2[i] = (72 * gg + 18 * (j >> 1) + (((j >> 1) & 1) ? 0 : 16) + (j & 1)) * 1152 + (cc % 9) * 16; }
#pragma unroll
    for (int i = 0; i < 4; ++i) {
      const int s = 4 * i + w; const unsigned dst = ldsb + L_WQ + s * BLK; const char* src = Wo + (size_t)s * 36 * 1152;
#pragma unroll
      for (int k = 0; k < 4; ++k) dma16(o1[k], src, dst + k * 1024);
      dma16_lo(o1[4], src, dst + 4096);
    }
    if (w < 2) {
      const unsigned dst = ldsb + L_WQ + (16 + w) * BLK; const char* src = Wo + (size_t)w * 288 * 1152;
#pragma unroll
      for (int k = 0; k < 4; ++k) dma16(o2[k], src, dst + k * 1024);
      dma16_lo(o2[4], src, dst + 4096);
    }
  }

  bf16x8 qb[2][5];
  bf16x8 Rq;
  if (pb == 0) {
    u32x2 pk[5][2];
#pragma unroll
    for (int i = 0; i < 5; ++i)
#pragma unroll
      for (int a = 0; a < 2; ++a) { const f32x4 x = qa[i][a]; pk[i][a] = pack4(f32x4{phiA(x[0]), phiA(x[1]), phiA(x[2]), phiA(x[3])}); }
#pragma unroll
    for (int P = 0; P < 2; ++P) { qb[P][0] = swap_pair(pk[2 * P][0], pk[2 * P][1]); qb[P][1] = swap_pair(pk[2 * P + 1][0], pk[2 * P + 1][1]); }
    Rq = swap_pair(pk[4][0], pk[4][1]);
  } else {
    Rq = swap_pair(pack4(qa[4][0]), pack4(qa[4][1]));
#pragma unroll
    for (int P = 0; P < 2; ++P) {
      const bf16x8 b0 = swap_pair(pack4(qa[2 * P][0]), pack4(qa[2 * P][1])), b1 = swap_pair(pack4(qa[2 * P + 1][0]), pack4(qa[2 * P + 1][1]));
      unsigned pp[8], pm[8], rp, rm;
      hedgehog(lds + L_PW + P * 6144, lane, b0, b1, Rq, pp, pm, rp, rm);
      qb[P][0] = __builtin_bit_cast(bf16x8, (u32x4{pp[0], pp[1], pp[2], pp[3]}));
      qb[P][1] = __builtin_bit_cast(bf16x8, (u32x4{pp[4], pp[5], pp[6], pp[7]}));
      qb[P][2] = __builtin_bit_cast(bf16x8, (u32x4{pm[0], pm[1], pm[2], pm[3]}));
      qb[P][3] = __builtin_bit_cast(bf16x8, (u32x4{pm[4], pm[5], pm[6], pm[7]}));
      qb[P][4] = __builtin_bit_cast(bf16x8, (u32x4{rp, rm, 0u, 0u}));
    }
  }
  STAMP(4);

  if (w == 0) wait_flags((const u64*)(ws + W_KVF + grp * 128), 16, tag, lane);
  LBAR();
  STAMP(5);
  {
    u32x4 v[3];
#pragma unroll
    for (int i = 0; i < 3; ++i) { int c = tid + 256 * i; if (c > (int)(KVG / 16) - 1) c = KVG / 16 - 1;
      asm volatile("global_load_dwordx4 %0, %1, off sc1" : "=v"(v[i]) : "v"(kvg + c * 16) : "memory"); }
    asm volatile("s_waitcnt vmcnt(0)" : "+v"(v[0]), "+v"(v[1]), "+v"(v[2]) :: "memory");
#pragma unroll
    for (int i = 0; i < 3; ++i) { int c = tid + 256 * i; if (c < (int)(KVG / 16)) *(LAS u32x4*)(lds + c * 16) = v[i]; }
  }
  LBAR();
  STAMP(6);

  char* ob = ws + W_O + ((size_t)(pb * 128 + tt * 8 + 2 * w + (t32 >> 4)) * 18) * 1024;
  const int hrow = (t32 >> 2) & 1, e0 = (t32 & 3) + 4 * (t32 >> 3), e3 = 16 + (t32 & 3);
  const bool v3 = t32 < 8 && (t32 & 3) < 3;
  LAS char* zero = lds + L_ZERO;
  unsigned orem[2];
#pragma unroll
  for (int P = 0; P < 2; ++P) {
    f32x16 O, O3;
    if (pb == 0) {
      LAS char* kv = lds + (2 * P + hrow) * (KVN_A * 4);
      LAS char* a = kv + (e0 * KVP_A + 8 * hh) * 4; LAS char* a3 = kv + (e3 * KVP_A + 8 * hh) * 4;
      const bf16x8 A0 = ldfrag8(hrow == 0 ? a : zero), A1 = ldfrag8(hrow == 1 ? a : zero);
      const bf16x8 A30 = ldfrag8(v3 && hrow == 0 ? a3 : zero), A31 = ldfrag8(v3 && hrow == 1 ? a3 : zero);
      const f32x2 r = *(LAS f32x2*)(hh == 0 ? kv + (e0 * KVP_A + 16) * 4 : zero), r3 = *(LAS f32x2*)(hh == 0 && v3 ? kv + (e3 * KVP_A + 16) * 4 : zero);
      const unsigned u = cvtpk(r[0], r[1]), u3 = cvtpk(r3[0], r3[1]);
      u32x4 tr = {0, 0, 0, 0}, tr3 = {0, 0, 0, 0};
      tr[2 * P] = hrow == 0 ? u : 0u; tr[2 * P + 1] = hrow == 1 ? u : 0u;
      tr3[2 * P] = hrow == 0 ? u3 : 0u; tr3[2 * P + 1] = hrow == 1 ? u3 : 0u;
      mfma32_0(O, A0, qb[P][0]); mfma32_0(O3, A30, qb[P][0]);
      mfma32(O, A1, qb[P][1]); mfma32(O3, A31, qb[P][1]);
      mfma32(O, __builtin_bit_cast(bf16x8, tr), Rq); mfma32(O3, __builtin_bit_cast(bf16x8, tr3), Rq);
    } else {
      LAS char* kv = lds + (2 * P + hh) * (KVN_B * 4);
      const bool act = hrow == hh;
      LAS char* a = act ? kv + e0 * KVP_B * 4 : zero; LAS char* a3 = act && v3 ? kv + e3 * KVP_B * 4 : zero;
      const int st = act ? 32 : 0, st3 = act && v3 ? 32 : 0;
      bf16x8 A[4], A3[4];
#pragma unroll
      for (int s = 0; s < 4; ++s) { A[s] = ldfrag8(a + s * st); A3[s] = ldfrag8(a3 + s * st3); }
      const f32x4 r = *(LAS f32x4*)(a + 4 * st), r3 = *(LAS f32x4*)(a3 + 4 * st3);
      const u32x4 tr = {cvtpk(r[0], r[1]), cvtpk(r[2], r[3]), 0u, 0u}, tr3 = {cvtpk(r3[0], r3[1]), cvtpk(r3[2], r3[3]), 0u, 0u};
      mfma32_0(O, A[0], qb[P][0]); mfma32_0(O3, A3[0], qb[P][0]);
#pragma unroll
      for (int s = 1; s < 4; ++s) { mfma32(O, A[s], qb[P][s]); mfma32(O3, A3[s], qb[P][s]); }
      mfma32(O, __builtin_bit_cast(bf16x8, tr), qb[P][4]); mfma32(O3, __builtin_bit_cast(bf16x8, tr3), qb[P][4]);
    }
    asm volatile("s_nop 7\n\ts_nop 3" : "+v"(O), "+v"(O3));
    const float rd = __builtin_amdgcn_rcpf(O3[2] + 1e-6f);
    u32x4 s0 = {cvtpk(O[0] * rd, O[1] * rd), cvtpk(O[2] * rd, O[3] * rd), cvtpk(O[4] * rd, O[5] * rd), cvtpk(O[6] * rd, O[7] * rd)};
    u32x4 s1 = {cvtpk(O[8] * rd, O[9] * rd), cvtpk(O[10] * rd, O[11] * rd), cvtpk(O[12] * rd, O[13] * rd), cvtpk(O[14] * rd, O[15] * rd)};
    orem[P] = cvtpk(O3[0] * rd, O3[1] * rd);
    char* f = ob + (size_t)(2 * hg + P) * 1024 + (2 * hh * 16 + t16) * 16;
    st16_sc1(f, s0); st16_sc1(f + 256, s1);
  }
  {
    auto r = __builtin_amdgcn_permlane32_swap(orem[0], orem[1], false, false);
    st8_sc1(ob + (size_t)(16 + (hg >> 2)) * 1024 + ((hg & 3) * 16 + t16) * 16 + 8 * hh, u32x2{r[0], r[1]});
  }
  WAITV(0);
  LBAR();
  if (tid == 0) __hip_atomic_store((GAS u64*)(ws + W_OF + (pb * 16 + tt) * 128 + hg * 8), tag, RLX_AGENT);
  STAMP(7);
  if (w == 0) wait_flags((const u64*)(ws + W_OF + (pb * 16 + tt) * 128), 8, tag, lane);
  LBAR();
  STAMP(8);

  {
    const char* of = ws + W_O + ((size_t)(pb * 128 + tt * 8 + 2 * w) * 18) * 1024 + lane * 16;
    u32x4 ofr[2][18];
#pragma unroll
    for (int s = 0; s < 18; ++s)
#pragma unroll
      for (int a = 0; a < 2; ++a)
        asm volatile("global_load_dwordx4 %0, %1, off sc1" : "=v"(ofr[a][s]) : "v"(of + (a * 18 + s) * 1024) : "memory");
    f32x4 oc[5][2];
#pragma unroll
    for (int i = 0; i < 5; ++i) { oc[i][0] = f32x4{0, 0, 0, 0}; oc[i][1] = f32x4{0, 0, 0, 0}; }
    const unsigned Lo = L_WQ + (4 * g + q4) * 288 + 8 * p4;
    auto rd = [&](int n) { const int s = n / 5, tl = n % 5; LAS char* pa = lds + s * BLK + Lo + tl * 32; return trfrag(pa, pa + 144); };
    bf16x8 wf[90];
    wf[0] = rd(0); wf[1] = rd(1);
    asm volatile("s_nop 1" : "+v"(oc[0][0]), "+v"(oc[0][1]), "+v"(oc[1][0]), "+v"(oc[1][1]), "+v"(oc[2][0]), "+v"(oc[2][1]), "+v"(oc[3][0]), "+v"(oc[3][1]), "+v"(oc[4][0]), "+v"(oc[4][1]));
    sfor<90>([&](auto N) {
      constexpr int n = N;
      if constexpr (n + 2 < 90) wf[n + 2] = rd(n + 2);
      if constexpr (n % 5 == 0) asm volatile("s_waitcnt vmcnt(%c2)" : "+v"(ofr[0][n / 5]), "+v"(ofr[1][n / 5]) : "i"(34 - 2 * (n / 5)) : "memory");
      GAP();
      mfma_vv(oc[n % 5][0], wf[n], __builtin_bit_cast(bf16x8, ofr[0][n / 5]));
      mfma_vv(oc[n % 5][1], wf[n], __builtin_bit_cast(bf16x8, ofr[1][n / 5]));
      GAP();
    });
    asm volatile("s_nop 7" : "+v"(oc[0][0]), "+v"(oc[0][1]), "+v"(oc[1][0]), "+v"(oc[1][1]), "+v"(oc[2][0]), "+v"(oc[2][1]), "+v"(oc[3][0]), "+v"(oc[3][1]), "+v"(oc[4][0]), "+v"(oc[4][1]));
    STAMP(9);
#pragma unroll
    for (int a = 0; a < 2; ++a) {
      char* orow = out + (size_t)(tt * 128 + 32 * w + 16 * a + t16) * 2304 + pb * 1152 + hg * 144;
#pragma unroll
      for (int pr = 0; pr < 2; ++pr) {
        const u32x2 n0 = pack4(oc[2 * pr][a]), n1 = pack4(oc[2 * pr + 1][a]);
        auto r0 = __builtin_amdgcn_permlane16_swap(n0[0], n1[0], false, false);
        auto r1 = __builtin_amdgcn_permlane16_swap(n0[1], n1[1], false, false);
        const int col = pr * 32 + (g & 1) * 16 + (g >> 1) * 8;
        *(u32x4*)(orow + col * 2) = u32x4{r0[0], r1[0], r0[1], r1[1]};
      }
      if (g < 2) *(u32x2*)(orow + (64 + 4 * g) * 2) = pack4(oc[4][a]);
    }
  }
  if (b == 0 && w == 0) {
    bool ok = true;
#pragma unroll
    for (int i = 0; i < 4; ++i) {
      const int idx = 64 * i + lane;
      const GAS u64* p = (const GAS u64*)(ws + W_OF + (idx >> 3) * 128 + (idx & 7) * 8);
      unsigned spins = 0;
#pragma nounroll
      for (; spins < SPIN_CAP; ++spins) { u64 v = __hip_atomic_load(p, RLX_AGENT); if (__all(v == tag)) break; __builtin_amdgcn_s_sleep(1); }
      ok = ok && spins < SPIN_CAP;
    }
    if (lane == 0) __hip_atomic_store((GAS u64*)(ws + W_EP), tag, RLX_AGENT);
  }
  STAMP(10);
}

extern "C" void kernel_launch(void* const* d_in, const int* in_sizes, int n_in, void* d_out, int out_size, void* d_ws, size_t ws_size, hipStream_t stream) {
  (void)hipFuncSetAttribute((const void*)fused_kernel, hipFuncAttributeMaxDynamicSharedMemorySize, LDS_BYTES);
  fused_kernel<<<256, 256, LDS_BYTES, stream>>>((const char*)d_in[0], (const char*)d_in[1], (const char*)d_in[2], (const char*)d_in[3], (const char*)d_in[4],
                                                (const char*)d_in[5], (const char*)d_in[6], (char*)d_out, (char*)d_ws);
}
```

```cpp
#include <hip/hip_runtime.h>
#include <stdint.h>
#include <utility>
#include <type_traits>

typedef __bf16 bf16x2 __attribute__((ext_vector_type(2)));
typedef __bf16 bf16x4 __attribute__((ext_vector_type(4)));
typedef __bf16 bf16x8 __attribute__((ext_vector_type(8)));
typedef float f32x2 __attribute__((ext_vector_type(2)));
typedef float f32x4 __attribute__((ext_vector_type(4)));
typedef float f32x16 __attribute__((ext_vector_type(16)));
typedef unsigned u32x2 __attribute__((ext_vector_type(2)));
typedef unsigned u32x4 __attribute__((ext_vector_type(4)));
typedef unsigned long long u64;
#define LAS __attribute__((address_space(3)))
#define GAS __attribute__((address_space(1)))
#define DI __device__ __forceinline__
#define GAP() __builtin_amdgcn_sched_barrier(0)
#define RLX_AGENT __ATOMIC_RELAXED, __HIP_MEMORY_SCOPE_AGENT

constexpr int BLK = 4608;
constexpr int KXO = 4 * BLK;
constexpr int KSLOT = KXO + 16384;
constexpr int L_WQ = 61440;
constexpr int L_WO = 6144;
constexpr int L_ZERO = L_WQ + 18 * BLK;
constexpr int L_DUMMY = L_ZERO + 512;
constexpr int L_CNT = L_ZERO + 448;
constexpr int L_PART = L_DUMMY + 512;
constexpr int L_WPHI = L_PART;
constexpr int L_PW = 151552;
static_assert(L_PART + 6144 <= L_PW && L_WO + 12 * BLK <= L_WQ, "LDS map");
static_assert(4 * KSLOT <= L_WQ + 17 * BLK, "Wq block 17 does not overlap the k/v ring");
constexpr int LDS_BYTES = 163840;
constexpr int PITCH_A = 288, PITCH_B = 480;
constexpr int KVP_A = 24, KVP_B = 40;
constexpr int KVN_A = 19 * KVP_A, KVN_B = 19 * KVP_B;

constexpr size_t W_EP = 0;
constexpr size_t W_KVF = 4096;
constexpr size_t W_OF = W_KVF + 16 * 16 * 128;
constexpr size_t W_PART = 131072;
constexpr size_t W_O = 2u << 20;
static_assert(W_OF + 32 * 8 * 128 <= W_PART && W_PART + 256 * 6144 <= W_O, "workspace map");
constexpr size_t W_STAMP = 8u << 20;
constexpr unsigned SPIN_CAP = 1u << 17;

#define STAMP(i) do { } while (0)
#define STAMPG(grp, i) do { } while (0)

DI void dma16(unsigned voff, const char* sbase, unsigned ldsaddr) {
  asm volatile("s_mov_b32 m0, %2\n\ts_nop 0\n\tglobal_load_lds_dwordx4 %0, %1" :: "v"(voff), "s"(sbase), "s"(ldsaddr) : "memory");
}
DI void dma16nt(unsigned voff, const char* sbase, unsigned ldsaddr) {
  asm volatile("s_mov_b32 m0, %2\n\ts_nop 0\n\tglobal_load_lds_dwordx4 %0, %1 nt" :: "v"(voff), "s"(sbase), "s"(ldsaddr) : "memory");
}
DI void dma16_lo(unsigned voff, const char* sbase, unsigned ldsaddr) {
  asm volatile("s_mov_b32 m0, %2\n\ts_mov_b32 exec_hi, 0\n\tglobal_load_lds_dwordx4 %0, %1\n\ts_mov_b32 exec_hi, -1" :: "v"(voff), "s"(sbase), "s"(ldsaddr) : "memory");
}
DI void dma16_q(unsigned voff, const char* sbase, unsigned ldsaddr) {
  asm volatile("s_mov_b32 m0, %2\n\ts_mov_b64 exec, 0xffff\n\tglobal_load_lds_dwordx4 %0, %1\n\ts_mov_b64 exec, -1" :: "v"(voff), "s"(sbase), "s"(ldsaddr) : "memory");
}
DI bf16x8 trfrag(LAS char* p0, LAS char* p1) {
  bf16x4 a = __builtin_amdgcn_ds_read_tr16_b64_v4bf16((LAS bf16x4*)p0);
  bf16x4 b = __builtin_amdgcn_ds_read_tr16_b64_v4bf16((LAS bf16x4*)p1);
  return __builtin_shufflevector(a, b, 0, 1, 2, 3, 4, 5, 6, 7);
}
DI void mfma_vv(f32x4& d, const bf16x8& a, const bf16x8& b) {
  asm volatile("v_mfma_f32_16x16x32_bf16 %0, %1, %2, %0" : "+v"(d) : "v"(a), "v"(b));
}
DI void mfma_vn(f32x4& d, const bf16x8& a, const bf16x8& b) {
  asm volatile("s_nop 1\n\tv_mfma_f32_16x16x32_bf16 %0, %1, %2, %0" : "+v"(d) : "v"(a), "v"(b));
}
DI void mfma_vv00(f32x4& d, const bf16x8& a, const bf16x8& b) {
  asm volatile("v_mfma_f32_16x16x32_bf16 %0, %1, %2, 0" : "=&v"(d) : "v"(a), "v"(b));
}
DI void mfma_vv0(f32x4& d, const bf16x8& a, const bf16x8& b) {
  asm volatile("s_nop 1\n\tv_mfma_f32_16x16x32_bf16 %0, %1, %2, 0" : "=&v"(d) : "v"(a), "v"(b));
}
DI void mfma32(f32x16& d, const bf16x8& a, const bf16x8& b) {
  asm volatile("s_nop 1\n\tv_mfma_f32_32x32x16_bf16 %0, %1, %2, %0" : "+v"(d) : "v"(a), "v"(b));
}
DI void mfma32_0(f32x16& d, const bf16x8& a, const bf16x8& b) {
  asm volatile("s_nop 1\n\tv_mfma_f32_32x32x16_bf16 %0, %1, %2, 0" : "=&v"(d) : "v"(a), "v"(b));
}
template <int... I, class F> DI void sfor_(std::integer_sequence<int, I...>, F&& f) { (f(std::integral_constant<int, I>{}), ...); }
template <int N, class F> DI void sfor(F&& f) { sfor_(std::make_integer_sequence<int, N>{}, f); }
#define WAITV(n) asm volatile("s_waitcnt vmcnt(" #n ")" ::: "memory")
#define WAITL0() asm volatile("s_waitcnt lgkmcnt(0)" ::: "memory")
#define BAR() asm volatile("s_barrier" ::: "memory")
#define LBAR() asm volatile("s_waitcnt lgkmcnt(0)\n\ts_barrier" ::: "memory")

DI unsigned cvtpk(float lo, float hi) { f32x2 v = {lo, hi}; bf16x2 b = __builtin_convertvector(v, bf16x2); return __builtin_bit_cast(unsigned, b); }
DI u32x2 pack4(const f32x4& x) { return u32x2{cvtpk(x[0], x[1]), cvtpk(x[2], x[3])}; }
DI bf16x8 swap_pair(const u32x2& a, const u32x2& b) {
  auto r0 = __builtin_amdgcn_permlane16_swap(a[0], b[0], false, false);
  auto r1 = __builtin_amdgcn_permlane16_swap(a[1], b[1], false, false);
  u32x4 t = {r0[0], r1[0], r0[1], r1[1]};
  return __builtin_bit_cast(bf16x8, t);
}
DI float phiA(float t) {
  const float L2E = 1.4426950408889634f;
  float e = __builtin_amdgcn_exp2f(__builtin_fmaf(t, L2E, -1e-6f * L2E));
  e = __builtin_amdgcn_fmed3f(e, 0.f, 1.f);
  return __builtin_fmaxf(t, 1e-6f) + e;
}
DI bf16x8 ldfrag8(LAS char* p) {
  f32x4 a = *(LAS f32x4*)p, b = *(LAS f32x4*)(p + 16);
  u32x4 t = {cvtpk(a[0], a[1]), cvtpk(a[2], a[3]), cvtpk(b[0], b[1]), cvtpk(b[2], b[3])};
  return __builtin_bit_cast(bf16x8, t);
}
template <int IMM> DI void ld16_sc1(u32x4& d, unsigned voff, const char* sbase) {
  asm volatile("global_load_dwordx4 %0, %1, %2 offset:%c3 sc1" : "=v"(d) : "v"(voff), "s"(sbase), "i"(IMM) : "memory");
}
template <int IMM> DI void st16_sc1(unsigned voff, char* sbase, const u32x4& v) {
  asm volatile("global_store_dwordx4 %0, %1, %2 offset:%c3 sc1\n\ts_nop 1" :: "v"(voff), "v"(v), "s"(sbase), "i"(IMM) : "memory");
}
DI void st8_sc1(unsigned voff, char* sbase, const u32x2& v) {
  asm volatile("global_store_dwordx2 %0, %1, %2 sc1" :: "v"(voff), "v"(v), "s"(sbase) : "memory");
}

DI void hh_mfma(LAS char* pw, const bf16x8& b0, const bf16x8& b1, const bf16x8& br, f32x16& D, f32x16& D2) {
  const bf16x8 w0 = *(LAS bf16x8*)(pw), w3 = *(LAS bf16x8*)(pw + 3072), w1 = *(LAS bf16x8*)(pw + 1024), w4 = *(LAS bf16x8*)(pw + 4096);
  GAP();
  mfma32_0(D, w0, b0); mfma32_0(D2, w3, b0);
  const bf16x8 w2 = *(LAS bf16x8*)(pw + 2048), w5 = *(LAS bf16x8*)(pw + 5120);
  GAP();
  mfma32(D, w1, b1); mfma32(D2, w4, b1);
  mfma32(D, w2, br); mfma32(D2, w5, br);
}
template <class H> DI void hh_softmax(const f32x16& D, const f32x16& D2, unsigned (&pp)[8], unsigned (&pm)[8], unsigned& rp, unsigned& rm, H&& hook) {
  float x[18];
#pragma unroll
  for (int i = 0; i < 16; ++i) x[i] = D[i];
  x[16] = D2[0]; x[17] = D2[1];
  float M = x[0], m = x[0];
#pragma unroll
  for (int i = 1; i < 18; ++i) { M = __builtin_fmaxf(M, x[i]); m = __builtin_fminf(m, x[i]); }
  const float L2E = 1.4426950408889634f;
  const f32x2 sc = {L2E, -L2E};
  f32x2 off = {-M * L2E, m * L2E};
  asm volatile("" : "+v"(off));
  hook(0);
  f32x2 e[18], sum = {0.f, 0.f};
#pragma unroll
  for (int i = 0; i < 18; ++i) {
    const f32x2 a = __builtin_elementwise_fma(f32x2{x[i], x[i]}, sc, off);
    e[i] = f32x2{__builtin_amdgcn_exp2f(a[0]), __builtin_amdgcn_exp2f(a[1])};
    sum += e[i];
    if (i == 5 || i == 11) { asm volatile("" : "+v"(sum)); hook(i == 5 ? 1 : 2); }
  }
  f32x2 rn = {__builtin_amdgcn_rcpf(sum[0]), __builtin_amdgcn_rcpf(sum[1])};
  asm volatile("" : "+v"(rn));
  hook(3);
#pragma unroll
  for (int i = 0; i < 18; ++i) e[i] *= rn;
#pragma unroll
  for (int i = 0; i < 8; ++i) { pp[i] = cvtpk(e[2 * i][0], e[2 * i + 1][0]); pm[i] = cvtpk(e[2 * i][1], e[2 * i + 1][1]); }
  rp = cvtpk(e[16][0], e[17][0]); rm = cvtpk(e[16][1], e[17][1]);
}
DI void touch(unsigned voff, const char* sbase, unsigned ldsaddr, unsigned long long mask) {
  asm volatile("s_mov_b32 m0, %2\n\ts_mov_b64 exec, %3\n\tglobal_load_lds_dword %0, %1\n\ts_mov_b64 exec, -1" :: "v"(voff), "s"(sbase), "s"(ldsaddr), "s"(mask) : "memory");
}
DI void touch_nt(unsigned voff, const char* sbase, unsigned ldsaddr, unsigned long long mask) {
  asm volatile("s_mov_b32 m0, %2\n\ts_mov_b64 exec, %3\n\tglobal_load_lds_dword %0, %1 nt\n\ts_mov_b64 exec, -1" :: "v"(voff), "s"(sbase), "s"(ldsaddr), "s"(mask) : "memory");
}
typedef __bf16 bf16x2_ __attribute__((ext_vector_type(2)));
DI float add_half(unsigned u, unsigned sel, float c) { return __builtin_amdgcn_fdot2_f32_bf16(__builtin_bit_cast(bf16x2_, u), __builtin_bit_cast(bf16x2_, sel), c, false); }
DI void pin(u64& x) { asm volatile("" : "+v"(x)); }
DI void ld8_sc1(u64& d, const char* p) { asm volatile("global_load_dwordx2 %0, %1, off sc1" : "=v"(d) : "v"(p) : "memory"); }

DI void lds_arrive(LAS char* word, int lane) {
  asm volatile("s_waitcnt lgkmcnt(0)" ::: "memory");
  if (lane == 0) __hip_atomic_fetch_add((LAS unsigned*)word, 1u, __ATOMIC_RELAXED, __HIP_MEMORY_SCOPE_WORKGROUP);
}
DI void lds_wait(LAS char* word, unsigned target) {
#pragma nounroll
  for (unsigned spins = 0; spins < SPIN_CAP * 8; ++spins) {
    if (__builtin_amdgcn_readfirstlane(__hip_atomic_load((LAS unsigned*)word, __ATOMIC_RELAXED, __HIP_MEMORY_SCOPE_WORKGROUP)) >= target) break;
    __builtin_amdgcn_s_sleep(1);
  }
  asm volatile("" ::: "memory");
}
DI void wait_flags_at(const u64* f, u64 tag) {
  const GAS u64* p = (const GAS u64*)f;
#pragma nounroll
  for (unsigned spins = 0; spins < SPIN_CAP; ++spins) {
    u64 v = __hip_atomic_load(p, RLX_AGENT);
    if (__all(v == tag)) return;
    __builtin_amdgcn_s_sleep(1);
  }
}
DI bool wait_flags(const u64* f, int n, u64 tag, int lane) {
  const GAS u64* p = (const GAS u64*)f + (lane < n ? lane : 0);
#pragma nounroll
  for (unsigned spins = 0; spins < SPIN_CAP; ++spins) {
    u64 v = __hip_atomic_load(p, RLX_AGENT);
    if (__all(v == tag)) return true;
    __builtin_amdgcn_s_sleep(1);
  }
  return false;
}

#define LANE_VARS int tid_ = threadIdx.x; asm volatile("" : "+v"(tid_)); const int tid = tid_, lane = tid & 63, g = lane >> 4, q4 = (lane & 15) >> 2, p4 = lane & 3, t16 = lane & 15, t32 = lane & 31, hh = lane >> 5; \
  (void)g; (void)q4; (void)p4; (void)t16; (void)t32; (void)hh
__global__ __launch_bounds__(512, 2) void fused_kernel(const char* __restrict__ xa, const char* __restrict__ wa, const char* __restrict__ woa,
                                                       const char* __restrict__ xb, const char* __restrict__ wb, const char* __restrict__ wphi,
                                                       const char* __restrict__ wob, char* __restrict__ out, char* __restrict__ ws) {
  asm volatile(".Lfk_begin:" ::: "memory");
  extern __shared__ __attribute__((aligned(16))) char lds_[];
  LAS char* lds = (LAS char*)lds_;
  const unsigned ldsb = (unsigned)(size_t)lds_;
  const int w = __builtin_amdgcn_readfirstlane(threadIdx.x >> 6);
  const int tg = w & 3;
  const bool qrole = w >= 4;
  const int b = blockIdx.x, xcd = b & 7, r = b >> 3;
  const int pb = (xcd >> 2) ^ 1;
  const int tt = (xcd & 3) * 4 + (r >> 3), hg = r & 7;
  const char* X = (pb ? xb : xa) + (size_t)(tt * 128 + 32 * tg) * 1152;
  const char* W = (pb ? wb : wa) + hg * 144;
  const char* Wo = (pb ? wob : woa) + hg * 144;
  const int grp = pb * 8 + hg;
  const int c0 = (2 * (tt & 3) + 5 * (tt >> 2)) % 9;
  auto rot = [&](int c) { const int a = c + c0; return a >= 9 ? a - 9 : a; };
  STAMP(0);
  const unsigned ldum = ldsb + L_DUMMY;
  const float ini = pb ? 0.f : 1e-6f;
  u64 epf[4] = {0, 0, 0, 0};
  u64 tag;
  auto wo_offsets = [&](unsigned (&o1)[5], int lane) {
#pragma unroll
    for (int i = 0; i < 5; ++i) { unsigned c = 64 * i + lane; c = c < 288 ? c : 287; const unsigned row = (c * 3641u) >> 16, cc = c - 18 * row, half = cc >= 9, gg = row >> 2, j = 4 * half + (row & 3);
      o1[i] = (20 * (gg >> 1) + 8 * (gg & 1) + j) * 1152 + (cc - 9 * half) * 16; } };
  auto wo_block = [&](const unsigned (&o1)[5], int s) {
    const unsigned dst = ldsb + L_WO + s * BLK; const char* src = Wo + (unsigned)s * (36u * 1152u);
#pragma unroll
    for (int i = 0; i < 4; ++i) dma16(o1[i], src, dst + i * 1024);
    dma16_lo(o1[4], src, dst + 4096); };

  if (qrole) {
    bf16x8 xf[2][18];
    f32x4 qa[5][2];
    {
      LANE_VARS;
      const char* Xt = (pb ? xb : xa) + (size_t)(tt * 128 + 16 * hg) * 1152;
      const unsigned txo = lane * 1152u;
      auto touch_x = [&](int c) { touch_nt(txo, Xt + rot(c) * 128, ldum, 0xffffull); };
      const char* pc; int back; unsigned len;
      asm volatile("s_getpc_b64 %0\n.Lfk_here:\n\ts_mov_b32 %1, .Lfk_begin-.Lfk_here\n\ts_mov_b32 %2, .Lfk_end-.Lfk_begin" : "=s"(pc), "=s"(back), "=s"(len));
      pc += back;
      const unsigned code0 = (lane * 32 + r) * 128u < len ? (lane * 32 + r) * 128u : 0u;
      if (w == 7) {
        touch(code0, pc, ldum, 0xffffull);
      } else { int c = 64 * tg + lane; if (c > 161) c = 161; dma16(c * 16, wphi + hg * 2592, ldsb + L_WPHI + tg * 1024); }
      touch_x(tg);
      unsigned xoff[4], woff[5], qoff[5];
#pragma unroll
      for (int i = 0; i < 4; ++i) { const int row = 8 * i + (lane >> 3); const int f = ((row & 15) >> 1) & 7; xoff[i] = row * 1152 + (((lane & 7) ^ f) << 4); }
#pragma unroll
      for (int i = 0; i < 5; ++i) { unsigned c = 64 * i + lane; c = c < 288 ? c : 287; const unsigned row = (c * 3641u) >> 16, cc = c - 18 * row, half = cc >= 9, rowk = 8 * (row >> 2) + (row & 3), pc = (cc - 9 * half) * 16;
        woff[i] = (32 * (tg >> 1) + 4 * (tg & 1) + rowk) * 3456 + (1 + half) * 1152 + pc;
        qoff[i] = (rowk + 4 * half) * 3456 + pc; }
      auto stage = [&](int c) {
        const unsigned slot = ldsb + (c & 3) * KSLOT; const int ca = rot(c);
        const char* xs = X + ca * 128; const char* wsrc = W + (unsigned)ca * (64u * 3456u);
#pragma unroll
        for (int i = 0; i < 4; ++i) dma16(xoff[i], xs, slot + KXO + tg * 4096 + i * 1024);
#pragma unroll
        for (int i = 0; i < 4; ++i) dma16(woff[i], wsrc, slot + tg * BLK + i * 1024);
        dma16_lo(woff[4], wsrc, slot + tg * BLK + 4096); };
      auto qblock = [&](int s) {
        const unsigned dst = ldsb + L_WQ + s * BLK; const char* src = W + (unsigned)(2 * rot(s >> 1) + (s & 1)) * (32u * 3456u);
#pragma unroll
        for (int i = 0; i < 4; ++i) dma16(qoff[i], src, dst + i * 1024);
        dma16_lo(qoff[4], src, dst + 4096); };
      stage(0);
      u64 ep;
      asm volatile("s_load_dwordx2 %0, %1, 0x0" : "=s"(ep) : "s"(ws + W_EP) : "memory");
      STAMPG(0, 11);
      WAITV(10);
      asm volatile("s_waitcnt lgkmcnt(0)\n\ts_barrier" : "+s"(ep) :: "memory");
      tag = ep + 1;
      stage(1);
      const unsigned Lx0 = KXO + tg * 4096 + t16 * 128 + ((g ^ ((t16 >> 1) & 7)) << 4), Lx1 = Lx0 ^ 64;
      auto rdx = [&](auto C) { constexpr int c = C; unsigned b0 = Lx0 + (c & 3) * KSLOT, b1 = Lx1 + (c & 3) * KSLOT; asm volatile("" : "+v"(b0), "+v"(b1));
        xf[0][2 * c] = *(LAS bf16x8*)(lds + b0); xf[1][2 * c] = *(LAS bf16x8*)(lds + b0 + 2048);
        xf[0][2 * c + 1] = *(LAS bf16x8*)(lds + b1); xf[1][2 * c + 1] = *(LAS bf16x8*)(lds + b1 + 2048); };
      WAITV(9);
      BAR();
      stage(2);
      rdx(std::integral_constant<int, 0>{});
      sfor<8>([&](auto C) {
        constexpr int c = C;
        if constexpr (c == 7) { if (tg < 2) WAITV(5); else WAITV(0); }
        else if constexpr (c >= 1 && c <= 5) { if (tg == ((c - 1) & 3)) WAITV(10); else WAITV(9); }
        else WAITV(9);
        LBAR();
        if constexpr (c <= 4) { if (tg == (c & 3)) touch_x(c + 4); }
        if constexpr (c + 3 <= 8) stage(c + 3);
        if constexpr (c == 6) { if (tg < 2) qblock(tg == 0 ? 0 : 17); }
        if constexpr (c == 7) { qblock(1 + 2 * tg); qblock(2 + 2 * tg); }
        rdx(std::integral_constant<int, c + 1>{});
      });
      LBAR();
      if (tg == 0) touch(lane * 128u, ws + W_PART + (size_t)(grp * 16 + tt) * 6144, ldum, 0xffffffffffffull);
      else if (tg == 1) touch((lane & 15) * 128u, ws + W_KVF + grp * 16 * 128, ldum, 0xffffull);
      else touch((lane & 7) * 128u, ws + W_OF + (pb * 16 + tt) * 8 * 128, ldum, 0xffull);
      qblock(9 + 2 * tg); qblock(10 + 2 * tg);
      { unsigned l = r * 162 + 41 * tg + lane; l = l < 5184 ? l : 5183; touch(l * 128u, pb ? wob : woa, ldum, 0x1ffffffffffull); }
#pragma unroll
      for (int i = 0; i < 5; ++i) { qa[i][0] = f32x4{ini, ini, ini, ini}; qa[i][1] = f32x4{ini, ini, ini, ini}; }
      asm volatile("" : "+v"(qa[0][0]), "+v"(qa[0][1]), "+v"(qa[1][0]), "+v"(qa[1][1]), "+v"(qa[2][0]), "+v"(qa[2][1]), "+v"(qa[3][0]), "+v"(qa[3][1]), "+v"(qa[4][0]), "+v"(qa[4][1]));
      WAITV(12);
      lds_arrive(lds + L_CNT + 0, lane); lds_wait(lds + L_CNT + 0, 4);
    }
    STAMPG(3, 11);
    {
      LANE_VARS;
      const unsigned rowb = (4 * g + q4) * 288;
      unsigned bq[2] = {L_WQ + rowb + 8 * p4, L_WQ + 9 * BLK + rowb + 8 * p4}, bqr[2] = {L_WQ + rowb + 32 + 72 * (p4 & 1), L_WQ + 9 * BLK + rowb + 32 + 72 * (p4 & 1)};
      asm volatile("" : "+v"(bq[0]), "+v"(bq[1]), "+v"(bqr[0]), "+v"(bqr[1]));
      auto ks = [](int i) { return i == 0 ? 17 : i - 1; };
      auto rd = [&](int n) { const int s = ks(n / 5), tl = n % 5, hi = s >= 9; constexpr int moff[4] = {0, 40, 72, 112};
        LAS char* pa = lds + (tl < 4 ? bq[hi] : bqr[hi]) + ((s - 9 * hi) * BLK + (tl < 4 ? moff[tl] : 0)); return trfrag(pa, pa + 144); };
      bf16x8 wf[90];
      wf[0] = rd(0); wf[1] = rd(1);
      sfor<90>([&](auto N) {
        constexpr int n = N;
        if constexpr (n == 47) { WAITV(0); lds_arrive(lds + L_CNT + 20, lane); lds_wait(lds + L_CNT + 20, 4); }
        if constexpr (n + 2 < 90) wf[n + 2] = rd(n + 2);
        GAP();
        mfma_vv(qa[n % 5][0], wf[n], xf[0][n / 5 == 0 ? 17 : n / 5 - 1]);
        mfma_vv(qa[n % 5][1], wf[n], xf[1][n / 5 == 0 ? 17 : n / 5 - 1]);
        GAP();
      });
      asm volatile("s_nop 7" : "+v"(qa[0][0]), "+v"(qa[0][1]), "+v"(qa[1][0]), "+v"(qa[1][1]), "+v"(qa[2][0]), "+v"(qa[2][1]), "+v"(qa[3][0]), "+v"(qa[3][1]), "+v"(qa[4][0]), "+v"(qa[4][1]));
    }
    STAMPG(3, 12);
    bf16x8 qb[2][5];
    bf16x8 Rq;
    {
      LANE_VARS;
#pragma unroll
      for (int i = 0; i < 2; ++i) touch((lane < 16 ? 2 * hg * 1024 + (lane & 15) * 128 : (16 + (hg >> 2)) * 1024 + (hg & 3) * 256 + (lane & 1) * 128),
                                        ws + W_O + ((size_t)(pb * 128 + tt * 8 + 2 * tg + i) * 18) * 1024, ldum, 0x3ffffull);
      if (pb == 0) {
        u32x2 pk[5][2];
#pragma unroll
        for (int i = 0; i < 5; ++i)
#pragma unroll
          for (int a = 0; a < 2; ++a) { const f32x4 x = qa[i][a]; pk[i][a] = pack4(f32x4{phiA(x[0]), phiA(x[1]), phiA(x[2]), phiA(x[3])}); }
#pragma unroll
        for (int P = 0; P < 2; ++P) { qb[P][0] = swap_pair(pk[2 * P][0], pk[2 * P][1]); qb[P][1] = swap_pair(pk[2 * P + 1][0], pk[2 * P + 1][1]); }
        Rq = swap_pair(pk[4][0], pk[4][1]);
      } else {
        Rq = swap_pair(pack4(qa[4][0]), pack4(qa[4][1]));
        f32x16 D[2], D2[2];
#pragma unroll
        for (int P = 0; P < 2; ++P) {
          const bf16x8 b0 = swap_pair(pack4(qa[2 * P][0]), pack4(qa[2 * P][1])), b1 = swap_pair(pack4(qa[2 * P + 1][0]), pack4(qa[2 * P + 1][1]));
          hh_mfma(lds + L_PW + P * 6144 + lane * 16, b0, b1, Rq, D[P], D2[P]);
        }
        asm volatile("s_nop 7\n\ts_nop 3" : "+v"(D[0]), "+v"(D2[0]), "+v"(D[1]), "+v"(D2[1]));
#pragma unroll
        for (int P = 0; P < 2; ++P) {
          unsigned pp[8], pm[8], rp, rm;
          hh_softmax(D[P], D2[P], pp, pm, rp, rm, [](int) {});
          qb[P][0] = __builtin_bit_cast(bf16x8, (u32x4{pp[0], pp[1], pp[2], pp[3]}));
          qb[P][1] = __builtin_bit_cast(bf16x8, (u32x4{pp[4], pp[5], pp[6], pp[7]}));
          qb[P][2] = __builtin_bit_cast(bf16x8, (u32x4{pm[0], pm[1], pm[2], pm[3]}));
          qb[P][3] = __builtin_bit_cast(bf16x8, (u32x4{pm[4], pm[5], pm[6], pm[7]}));
          qb[P][4] = __builtin_bit_cast(bf16x8, (u32x4{rp, rm, 0u, 0u}));
        }
      }
      asm volatile("" : "+v"(qb[0][0]), "+v"(qb[0][1]), "+v"(qb[1][0]), "+v"(qb[1][1]));
    }
    STAMPG(3, 13);
    LBAR();
    STAMPG(3, 14);
    {
      LANE_VARS;
      char* ob = ws + W_O + ((size_t)(pb * 128 + tt * 8 + 2 * tg) * 18) * 1024;
      const int hrow = (t32 >> 2) & 1, e0 = (t32 & 3) + 4 * (t32 >> 3), e3 = 16 + (t32 & 3);
      const bool v3 = t32 < 8 && (t32 & 3) < 3;
      f32x16 O[2], O3[2];
      if (pb == 0) {
#pragma unroll
        for (int P = 0; P < 2; ++P) {
          const unsigned kv = (2 * P + hrow) * (KVN_A * 2);
          const unsigned a = kv + e0 * (KVP_A * 2) + 16 * hh, a3 = kv + e3 * (KVP_A * 2) + 16 * hh;
          const bf16x8 A0 = *(LAS bf16x8*)(lds + (hrow == 0 ? a : L_ZERO)), A1 = *(LAS bf16x8*)(lds + (hrow == 1 ? a : L_ZERO));
          const bf16x8 A30 = *(LAS bf16x8*)(lds + (v3 && hrow == 0 ? a3 : L_ZERO)), A31 = *(LAS bf16x8*)(lds + (v3 && hrow == 1 ? a3 : L_ZERO));
          const unsigned u = *(LAS unsigned*)(lds + (hh == 0 ? kv + e0 * (KVP_A * 2) + 32 : L_ZERO)), u3 = *(LAS unsigned*)(lds + (hh == 0 && v3 ? kv + e3 * (KVP_A * 2) + 32 : L_ZERO));
          u32x4 tr = {0u, 0u, 0u, 0u}, tr3 = {0u, 0u, 0u, 0u};
          tr[2 * P] = hrow == 0 ? u : 0u; tr[2 * P + 1] = hrow == 1 ? u : 0u;
          tr3[2 * P] = hrow == 0 ? u3 : 0u; tr3[2 * P + 1] = hrow == 1 ? u3 : 0u;
          mfma32_0(O[P], A0, qb[P][0]); mfma32_0(O3[P], A30, qb[P][0]);
          mfma32(O[P], A1, qb[P][1]); mfma32(O3[P], A31, qb[P][1]);
          mfma32(O[P], __builtin_bit_cast(bf16x8, tr), Rq); mfma32(O3[P], __builtin_bit_cast(bf16x8, tr3), Rq);
        }
      } else {
#pragma unroll
        for (int P = 0; P < 2; ++P) {
          const bool act = hrow == hh;
          const unsigned kv = (2 * P + hh) * (KVN_B * 2);
          LAS char* a = lds + (act ? kv + e0 * (KVP_B * 2) : L_ZERO); LAS char* a3 = lds + (act && v3 ? kv + e3 * (KVP_B * 2) : L_ZERO);
          bf16x8 A[5], A3[5];
#pragma unroll
          for (int s = 0; s < 5; ++s) { A[s] = *(LAS bf16x8*)(a + 16 * s); A3[s] = *(LAS bf16x8*)(a3 + 16 * s); }
          mfma32_0(O[P], A[0], qb[P][0]); mfma32_0(O3[P], A3[0], qb[P][0]);
#pragma unroll
          for (int s = 1; s < 5; ++s) { mfma32(O[P], A[s], qb[P][s]); mfma32(O3[P], A3[s], qb[P][s]); }
        }
      }
      asm volatile("s_nop 7\n\ts_nop 3" : "+v"(O[0]), "+v"(O3[0]), "+v"(O[1]), "+v"(O3[1]));
      unsigned orem[2];
      const unsigned tile = (t32 >> 4) * (18 * 1024);
#pragma unroll
      for (int P = 0; P < 2; ++P) {
        const float rd = __builtin_amdgcn_rcpf(O3[P][2] + 1e-6f);
        const f32x16& o = O[P];
        const u32x4 s0 = {cvtpk(o[0] * rd, o[1] * rd), cvtpk(o[2] * rd, o[3] * rd), cvtpk(o[4] * rd, o[5] * rd), cvtpk(o[6] * rd, o[7] * rd)};
        const u32x4 s1 = {cvtpk(o[8] * rd, o[9] * rd), cvtpk(o[10] * rd, o[11] * rd), cvtpk(o[12] * rd, o[13] * rd), cvtpk(o[14] * rd, o[15] * rd)};
        orem[P] = cvtpk(O3[P][0] * rd, O3[P][1] * rd);
        const unsigned fo = tile + (2 * hg + P) * 1024 + (2 * hh * 16 + t16) * 16;
        st16_sc1<0>(fo, ob, s0); st16_sc1<256>(fo, ob, s1);
      }
      auto rr = __builtin_amdgcn_permlane32_swap(orem[0], orem[1], false, false);
      st8_sc1(tile + (16 + (hg >> 2)) * 1024 + ((hg & 3) * 16 + t16) * 16 + 8 * hh, ob, u32x2{rr[0], rr[1]});
    }
    STAMPG(3, 15);
  } else {
    f32x4 acc[9][2];
    {
      LANE_VARS;
      u64 ep;
      asm volatile("s_load_dwordx2 %0, %1, 0x0" : "=s"(ep) : "s"(ws + W_EP) : "memory");
      const float inir = g < 2 ? ini : 0.f;
#pragma unroll
      for (int i = 0; i < 9; ++i) { const float v = i < 4 ? ini : (i < 8 ? 0.f : inir); acc[i][0] = f32x4{v, v, v, v}; acc[i][1] = f32x4{v, v, v, v}; }
      asm volatile("" : "+v"(acc[0][0]), "+v"(acc[0][1]), "+v"(acc[1][0]), "+v"(acc[1][1]), "+v"(acc[2][0]), "+v"(acc[2][1]), "+v"(acc[3][0]), "+v"(acc[3][1]), "+v"(acc[4][0]),
                        "+v"(acc[4][1]), "+v"(acc[5][0]), "+v"(acc[5][1]), "+v"(acc[6][0]), "+v"(acc[6][1]), "+v"(acc[7][0]), "+v"(acc[7][1]), "+v"(acc[8][0]), "+v"(acc[8][1]));
      if (tid < 128) *(LAS unsigned*)(lds + L_ZERO + tid * 4) = 0u;
      asm volatile("s_waitcnt lgkmcnt(0)\n\ts_barrier" : "+s"(ep) :: "memory");
      tag = ep + 1;
      if (pb) {
#pragma unroll
        for (int i = 0; i < 3; ++i) {
          const int f = 3 * w + i, PP = f >= 6, kind = f - 6 * PP, d2 = kind >= 3, k3 = kind - 3 * d2;
          const int hrow = (t32 >> 2) & 1;
          const int fo = d2 ? 16 + (t32 & 3) : (t32 & 3) + 4 * (t32 >> 3);
          const bool valid = d2 ? (t32 < 8 && (t32 & 3) < 2) : true;
          u32x4 t;
          if (k3 < 2) {
            LAS char* p = lds + ((valid && hrow == k3) ? L_WPHI + (((2 * PP + k3) * 18 + 8 * hh + 2 * k3) * 18 + fo) * 2 : L_ZERO);
#pragma unroll
            for (int j = 0; j < 4; ++j) t[j] = *(LAS unsigned short*)(p + 72 * j) | (unsigned)*(LAS unsigned short*)(p + 72 * j + 36) << 16;
          } else {
            LAS char* p = lds + ((valid && hh == 0) ? L_WPHI + (((2 * PP + hrow) * 18 + (hrow ? 0 : 16)) * 18 + fo) * 2 : L_ZERO);
            const unsigned u = *(LAS unsigned short*)(p) | (unsigned)*(LAS unsigned short*)(p + 36) << 16;
            const int dw = 2 * PP + hrow;
            t = u32x4{dw == 0 ? u : 0u, dw == 1 ? u : 0u, dw == 2 ? u : 0u, dw == 3 ? u : 0u};
          }
          *(LAS u32x4*)(lds + L_PW + f * 1024 + lane * 16) = t;
        }
      }
      STAMPG(0, 12);
      constexpr int MB = 10;
      const unsigned rowb = (4 * g + q4) * 288;
      const unsigned Lkv = rowb + 8 * p4, Lrem = rowb + 32 + 72 * p4;
      const unsigned Lx0 = KXO + tg * 4096 + t16 * 128 + ((g ^ ((t16 >> 1) & 7)) << 4), Lx1 = Lx0 ^ 64;
      unsigned bm[9], br[9], bx0[9], bx1[9];
      auto bases = [&](auto C) { constexpr int c = C, sl = (c & 3) * KSLOT;
        bm[c] = Lkv + sl; br[c] = Lrem + sl; bx0[c] = Lx0 + sl; bx1[c] = Lx1 + sl;
        asm volatile("" : "+v"(bm[c]), "+v"(br[c]), "+v"(bx0[c]), "+v"(bx1[c])); };
      auto rd = [&](int n) { const int c = n / 18, m = n % 18, s = m / 9, tl = m % 9; constexpr int moff[4] = {0, 40, 72, 112};
        LAS char* pa = lds + (tl < 8 ? bm[c] : br[c]) + (s * 2 * BLK + (tl < 8 ? (tl >> 2) * 144 + moff[tl & 3] : 0));
        return trfrag(pa, pa + BLK); };
      bf16x8 xf[2][18];
      auto rdx = [&](int c, int i) { xf[i & 1][2 * c + (i >> 1)] = *(LAS bf16x8*)(lds + ((i >> 1) ? bx1[c] : bx0[c]) + (i & 1) * 2048); };
      LBAR();
      STAMPG(0, 13);
      bases(std::integral_constant<int, 0>{});
      bf16x8 wf[162];
      rdx(0, 0); rdx(0, 1); wf[0] = rd(0); wf[1] = rd(1); rdx(0, 2); rdx(0, 3);
      sfor<162>([&](auto N) {
        constexpr int n = N, c = n / 18, m = n % 18;
        if constexpr (m == MB && c <= 7) {
          BAR();
          if constexpr (c == 3) STAMPG(0, 14);
          if constexpr (c == 7) STAMPG(0, 15);
          bases(std::integral_constant<int, c + 1>{});
        }
        if constexpr (n + 2 < 162) wf[n + 2] = rd(n + 2);
        if constexpr (c <= 7 && m > MB && m <= MB + 4) rdx(c + 1, m - MB - 1);
        GAP();
        mfma_vv(acc[m % 9][0], wf[n], xf[0][2 * c + m / 9]);
        mfma_vv(acc[m % 9][1], wf[n], xf[1][2 * c + m / 9]);
        GAP();
      });
      asm volatile("s_nop 7" : "+v"(acc[0][0]), "+v"(acc[0][1]), "+v"(acc[1][0]), "+v"(acc[1][1]), "+v"(acc[2][0]), "+v"(acc[2][1]), "+v"(acc[3][0]), "+v"(acc[3][1]), "+v"(acc[4][0]),
                               "+v"(acc[4][1]), "+v"(acc[5][0]), "+v"(acc[5][1]), "+v"(acc[6][0]), "+v"(acc[6][1]), "+v"(acc[7][0]), "+v"(acc[7][1]), "+v"(acc[8][0]), "+v"(acc[8][1]));
      LBAR();
    }
    STAMP(1);
    {
      LANE_VARS;
      const int sw16 = (t16 >> 2) & 3;
      const unsigned trp = (p4 ^ g) << 3;
      const unsigned dum = L_DUMMY + (lane & 31) * 16;
      const int h = tg;
      if (pb == 0) {
#pragma unroll
        for (int a = 0; a < 2; ++a) {
          const unsigned row = (32 * tg + 16 * a + t16) * PITCH_A + ((g ^ sw16) << 3);
#pragma unroll
          for (int e = 0; e < 4; ++e) {
            const f32x4 k = acc[e][a];
            *(LAS u32x2*)(lds + row + e * 32) = pack4(f32x4{phiA(k[0]), phiA(k[1]), phiA(k[2]), phiA(k[3])});
            *(LAS u32x2*)(lds + row + 128 + e * 32) = pack4(acc[4 + e][a]);
          }
          f32x4 rr = acc[8][a];
          const f32x4 ph = f32x4{phiA(rr[0]), phiA(rr[1]), phiA(rr[2]), phiA(rr[3])};
          if (g < 2) rr = ph;
          *(LAS u32x2*)(lds + row + 256) = pack4(rr);
        }
        STAMPG(1, 11);
        lds_arrive(lds + L_CNT + 4, lane); lds_wait(lds + L_CNT + 4, 4);
        STAMPG(1, 12);
        bf16x8 ones = __builtin_bit_cast(bf16x8, (u32x4{0x3f803f80u, 0x3f803f80u, 0x3f803f80u, 0x3f803f80u}));
        asm volatile("s_nop 1" : "+v"(ones));
        f32x4 kt[6];
        bf16x8 F0[4], E0[4], R[4];
#pragma unroll
        for (int u = 0; u < 4; ++u) {
          LAS char* base = lds + (32 * u + 4 * g + q4) * PITCH_A + trp;
          F0[u] = trfrag(base + h * 32, base + h * 32 + 16 * PITCH_A);
          E0[u] = trfrag(base + 128 + h * 32, base + 128 + h * 32 + 16 * PITCH_A);
          R[u] = trfrag(base + 256, base + 256 + 16 * PITCH_A);
        }
#pragma unroll
        for (int u = 0; u < 4; ++u) {
          if (u == 0) { mfma_vv0(kt[0], F0[u], E0[u]); mfma_vv0(kt[1], F0[u], R[u]); mfma_vv0(kt[2], R[u], E0[u]); mfma_vv0(kt[3], R[u], R[u]); mfma_vv0(kt[4], F0[u], ones); mfma_vv0(kt[5], R[u], ones); }
          else { mfma_vn(kt[0], F0[u], E0[u]); mfma_vn(kt[1], F0[u], R[u]); mfma_vn(kt[2], R[u], E0[u]); mfma_vn(kt[3], R[u], R[u]); mfma_vn(kt[4], F0[u], ones); mfma_vn(kt[5], R[u], ones); }
        }
        asm volatile("s_nop 7" : "+v"(kt[0]), "+v"(kt[1]), "+v"(kt[2]), "+v"(kt[3]), "+v"(kt[4]), "+v"(kt[5]));
        STAMPG(1, 13);
        const unsigned cp = L_PART + h * (KVN_A * 2);
        const int cc0 = 8 + 4 * (h >> 1) + 2 * (h & 1), cz = (cc0 + 2) & 15;
        const bool z = t16 == cz, sel = (t16 == cc0) | (t16 == cc0 + 1) | z, odd = h & 1;
        const bool gs = g == (h >> 1), gz = g == ((h >> 1) ^ 1);
        const int erow = z ? 18 : 16 + (t16 - cc0);
        const f32x4 m1 = z ? kt[4] : kt[1], m2 = z ? kt[5] : kt[3];
        const unsigned u0 = odd ? cvtpk(kt[2][2], kt[2][3]) : cvtpk(kt[2][0], kt[2][1]), u1 = odd ? cvtpk(m2[2], m2[3]) : cvtpk(m2[0], m2[1]);
        *(LAS u32x2*)(lds + cp + (t16 * KVP_A + 4 * g) * 2) = pack4(kt[0]);
        *(LAS u32x2*)(lds + (sel ? cp + (erow * KVP_A + 4 * g) * 2 : dum)) = pack4(m1);
        *(LAS u32x2*)(lds + (gs ? cp + (t16 * KVP_A + 16) * 2 : gz ? cp + (t16 * KVP_A + 20) * 2 : dum)) = u32x2{gs ? u0 : 0u, 0u};
        *(LAS u32x2*)(lds + (sel && gs ? cp + (erow * KVP_A + 16) * 2 : sel && gz ? cp + (erow * KVP_A + 20) * 2 : dum)) = u32x2{gs ? u1 : 0u, 0u};
      } else {
#pragma unroll
        for (int a = 0; a < 2; ++a) {
          const unsigned row = (32 * tg + 16 * a + t16) * PITCH_B;
#pragma unroll
          for (int e = 0; e < 4; ++e) *(LAS u32x2*)(lds + row + 256 + e * 32 + ((g ^ sw16) << 3)) = pack4(acc[4 + e][a]);
          const u32x2 vr = pack4(acc[8][a]);
          *(LAS u32x2*)(lds + row + 416 + (((g >= 2 ? g - 2 : g + 2) ^ sw16) << 3)) = g >= 2 ? vr : u32x2{g == 0 ? 0x3f80u : 0u, 0u};
        }
        const bf16x8 Rk = swap_pair(pack4(acc[8][0]), pack4(acc[8][1]));
        f32x16 D[2], D2[2];
#pragma unroll
        for (int P = 0; P < 2; ++P) {
          const bf16x8 b0 = swap_pair(pack4(acc[2 * P][0]), pack4(acc[2 * P][1])), b1 = swap_pair(pack4(acc[2 * P + 1][0]), pack4(acc[2 * P + 1][1]));
          hh_mfma(lds + L_PW + P * 6144 + lane * 16, b0, b1, Rk, D[P], D2[P]);
        }
        asm volatile("s_nop 7\n\ts_nop 3" : "+v"(D[0]), "+v"(D2[0]), "+v"(D[1]), "+v"(D2[1]));
        const int sw32 = (t32 >> 2) & 3;
        const unsigned row32 = (32 * tg + t32) * PITCH_B;
#pragma unroll
        for (int P = 0; P < 2; ++P) {
          unsigned pp[8], pm[8], rp, rm;
          hh_softmax(D[P], D2[P], pp, pm, rp, rm, [](int) {});
          LAS char* d = lds + row32 + (2 * P + hh) * 32;
#pragma unroll
          for (int pc = 0; pc < 4; ++pc) {
            *(LAS u32x2*)(d + ((pc ^ sw32) << 3)) = u32x2{pp[2 * pc], pp[2 * pc + 1]};
            *(LAS u32x2*)(d + 128 + ((pc ^ sw32) << 3)) = u32x2{pm[2 * pc], pm[2 * pc + 1]};
          }
          *(LAS u32x2*)(lds + row32 + 384 + (((2 * P + hh) ^ sw32) << 3)) = u32x2{rp, rm};
        }
        STAMPG(1, 11);
        lds_arrive(lds + L_CNT + 4, lane); lds_wait(lds + L_CNT + 4, 4);
        STAMPG(1, 12);
        f32x4 kt[6];
        {
          constexpr int H2 = 16 * PITCH_B;
          const unsigned b1_ = (4 * g + q4) * PITCH_B + trp + h * 32, b2_ = (4 * g + q4) * PITCH_B + trp;
          bf16x8 Fp[4], Fm[4], E0[4], Fr[4], E1[4];
#pragma unroll
          for (int u = 0; u < 4; ++u) {
            LAS char* p1 = lds + b1_ + u * 32 * PITCH_B; LAS char* p2 = lds + b2_ + u * 32 * PITCH_B;
            Fp[u] = trfrag(p1, p1 + H2); Fm[u] = trfrag(p1 + 128, p1 + 128 + H2); E0[u] = trfrag(p1 + 256, p1 + 256 + H2);
            Fr[u] = trfrag(p2 + 384, p2 + 384 + H2); E1[u] = trfrag(p2 + 416, p2 + 416 + H2);
          }
#pragma unroll
          for (int u = 0; u < 4; ++u) {
            if (u == 0) { mfma_vv0(kt[0], Fp[u], E0[u]); mfma_vv0(kt[1], Fm[u], E0[u]); mfma_vv0(kt[2], Fr[u], E0[u]); mfma_vv0(kt[3], Fp[u], E1[u]); mfma_vv0(kt[4], Fm[u], E1[u]); mfma_vv0(kt[5], Fr[u], E1[u]); }
            else { mfma_vn(kt[0], Fp[u], E0[u]); mfma_vn(kt[1], Fm[u], E0[u]); mfma_vn(kt[2], Fr[u], E0[u]); mfma_vn(kt[3], Fp[u], E1[u]); mfma_vn(kt[4], Fm[u], E1[u]); mfma_vn(kt[5], Fr[u], E1[u]); }
          }
        }
        asm volatile("s_nop 7" : "+v"(kt[0]), "+v"(kt[1]), "+v"(kt[2]), "+v"(kt[3]), "+v"(kt[4]), "+v"(kt[5]));
        STAMPG(1, 13);
        const unsigned cp = L_PART + h * (KVN_B * 2);
        const int cc0 = 4 * (h >> 1) + 2 * (h & 1);
        const bool z = t16 == 8, sel = (t16 == cc0) | (t16 == cc0 + 1) | z, gs = g == h, gz = g == ((h + 1) & 3);
        const int erow = z ? 18 : 16 + (t16 - cc0);
        const unsigned a0 = cp + (t16 * KVP_B + 4 * g) * 2, a1 = cp + (erow * KVP_B + 4 * g) * 2;
        *(LAS u32x2*)(lds + a0) = pack4(kt[0]);
        *(LAS u32x2*)(lds + a0 + 32) = pack4(kt[1]);
        *(LAS u32x2*)(lds + (gs ? cp + (t16 * KVP_B + 32) * 2 : gz ? cp + (t16 * KVP_B + 36) * 2 : dum)) = gs ? pack4(kt[2]) : u32x2{0u, 0u};
        *(LAS u32x2*)(lds + (sel ? a1 : dum)) = pack4(kt[3]);
        *(LAS u32x2*)(lds + (sel ? a1 + 32 : dum)) = pack4(kt[4]);
        *(LAS u32x2*)(lds + (sel && gs ? cp + (erow * KVP_B + 32) * 2 : sel && gz ? cp + (erow * KVP_B + 36) * 2 : dum)) = gs ? pack4(kt[5]) : u32x2{0u, 0u};
      }
      STAMPG(1, 14);
      lds_arrive(lds + L_CNT + 8, lane); lds_wait(lds + L_CNT + 8, 4);
      char* pslot = ws + W_PART + (size_t)(grp * 16 + tt) * 6144;
      { const u32x4 v0 = *(LAS u32x4*)(lds + L_PART + tid * 16), v1 = *(LAS u32x4*)(lds + L_PART + 4096 + (tid & 127) * 16);
        st16_sc1<0>(tid * 16, pslot, v0);
        if (w < 2) st16_sc1<0>(4096 + tid * 16, pslot, v1); }
      STAMPG(1, 15);
      WAITV(0);
      unsigned old = 0;
      if (lane == 0) old = __hip_atomic_fetch_add((LAS unsigned*)(lds + L_CNT + 12), 1u, __ATOMIC_RELAXED, __HIP_MEMORY_SCOPE_WORKGROUP);
      if (__builtin_amdgcn_readfirstlane(old) == 3 && lane < 16) __hip_atomic_store((GAS u64*)(ws + W_KVF + (grp * 16 + lane) * 128 + tt * 8), tag, RLX_AGENT);
    }
    STAMP(2);
    unsigned o1[5];
    {
      LANE_VARS;
      wo_offsets(o1, lane);
      if (w != 0) { wo_block(o1, 2 * (w - 1)); wo_block(o1, 2 * (w - 1) + 1); }
      const char* kvf = ws + W_KVF + (grp * 16 + tt) * 128;
      if (w == 0) { wait_flags((const u64*)kvf, 16, tag, lane); lds_arrive(lds + L_CNT + 16, lane); }
      else lds_wait(lds + L_CNT + 16, 1);
      STAMP(3);
      const char* pbase = ws + W_PART + (size_t)grp * 16 * 6144;
      const unsigned ca = (96 * w + lane) * 16, cb = (96 * w + 64 + (lane & 31)) * 16;
      u32x4 pa[16], pc[16];
#pragma unroll
      for (int i = 0; i < 16; ++i) { ld16_sc1<0>(pa[i], ca, pbase + i * 6144); ld16_sc1<0>(pc[i], cb, pbase + i * 6144); }
      const unsigned one = 0x3f80u << (16 * (t16 & 1));
      const int dw = (t16 & 7) >> 1;
      const u32x4 sel = {dw == 0 ? one : 0u, dw == 1 ? one : 0u, dw == 2 ? one : 0u, dw == 3 ? one : 0u}, nil = {0u, 0u, 0u, 0u};
      bf16x8 I1 = __builtin_bit_cast(bf16x8, g == (t16 >> 3) ? sel : nil), I2 = __builtin_bit_cast(bf16x8, g == 2 + (t16 >> 3) ? sel : nil);
      asm volatile("s_nop 1" : "+v"(I1), "+v"(I2));
      f32x4 D1, D2, D3;
      sfor<16>([&](auto I) {
        constexpr int i = I;
        asm volatile("s_waitcnt vmcnt(%c2)" : "+v"(pa[i]), "+v"(pc[i]) : "i"(30 - 2 * i) : "memory");
        if constexpr (i == 0) { mfma_vv00(D1, I1, __builtin_bit_cast(bf16x8, pa[0])); mfma_vv00(D2, I2, __builtin_bit_cast(bf16x8, pa[0])); mfma_vv00(D3, I1, __builtin_bit_cast(bf16x8, pc[0])); }
        else { mfma_vv(D1, I1, __builtin_bit_cast(bf16x8, pa[i])); mfma_vv(D2, I2, __builtin_bit_cast(bf16x8, pa[i])); mfma_vv(D3, I1, __builtin_bit_cast(bf16x8, pc[i])); }
      });
      asm volatile("s_nop 7" : "+v"(D1), "+v"(D2), "+v"(D3));
      STAMP(4);
      const unsigned c1 = 96 * w + t16 + 16 * (g >> 1);
      *(LAS u32x2*)(lds + c1 * 16 + 8 * (g & 1)) = pack4(D1);
      *(LAS u32x2*)(lds + (c1 + 32) * 16 + 8 * (g & 1)) = pack4(D2);
      *(LAS u32x2*)(lds + (c1 + 64) * 16 + 8 * (g & 1)) = pack4(D3);
    }
    STAMP(5);
    LBAR();
    STAMP(6);
    {
      LANE_VARS;
      wo_block(o1, 6 + tg); wo_block(o1, 10 + tg);
      if (w >= 2) wo_block(o1, 12 + w);
      if (w < 2) {
#pragma unroll
        for (int i = 0; i < 5; ++i) { unsigned c = 64 * i + lane; c = c < 288 ? c : 287; const unsigned row = (c * 3641u) >> 16, cc = c - 18 * row, half = cc >= 9, gg = row >> 2, j = 4 * half + (row & 3);
          const unsigned o2 = (72 * gg + 18 * (j >> 1) + (((j >> 1) & 1) ? 0 : 16) + (j & 1)) * 1152 + (cc - 9 * half) * 16;
          const unsigned dst = ldsb + L_WO + (16 + w) * BLK; const char* src = Wo + (unsigned)w * (288u * 1152u);
          if (i < 4) dma16(o2, src, dst + i * 1024); else dma16_lo(o2, src, dst + 4096); }
      }
    }
  }
  WAITV(0);
  LBAR();
  {
    LANE_VARS;
    if (tid < 8) __hip_atomic_store((GAS u64*)(ws + W_OF + ((pb * 16 + tt) * 8 + tid) * 128 + hg * 8), tag, RLX_AGENT);
    STAMP(7);
    if (w == 0) wait_flags((const u64*)(ws + W_OF + ((pb * 16 + tt) * 8 + hg) * 128), 8, tag, lane);
    BAR();
    STAMP(8);

    const char* of = ws + W_O + ((size_t)(pb * 128 + tt * 8 + w) * 18) * 1024;
    constexpr int PD = 8;
    const unsigned lo = lane * 16;
    u32x4 ofr[18];
    auto oload = [&](auto S) { constexpr int s = S; ld16_sc1<(s & 3) * 1024>(ofr[s], lo, of + (s >> 2) * 4096); };
    sfor<PD>(oload);
    f32x4 oc[5];
#pragma unroll
    for (int i = 0; i < 5; ++i) oc[i] = f32x4{0, 0, 0, 0};
    unsigned bo[2] = {(unsigned)(L_WO + (4 * g + q4) * 288 + 8 * p4), (unsigned)(L_WO + 9 * BLK + (4 * g + q4) * 288 + 8 * p4)};
    asm volatile("" : "+v"(bo[0]), "+v"(bo[1]));
    auto rd = [&](int n) { const int s = n / 5, tl = n % 5, hi = s >= 9; LAS char* pa = lds + bo[hi] + ((s - 9 * hi) * BLK + tl * 32); return trfrag(pa, pa + 144); };
    constexpr int FD = 3;
    bf16x8 wf[90];
    sfor<FD>([&](auto N) { wf[N] = rd(N); });
    asm volatile("s_nop 1" : "+v"(oc[0]), "+v"(oc[1]), "+v"(oc[2]), "+v"(oc[3]), "+v"(oc[4]));
    sfor<90>([&](auto N) {
      constexpr int n = N;
      if constexpr (n + FD < 90) wf[n + FD] = rd(n + FD);
      if constexpr (n % 5 == 0) { constexpr int s = n / 5, young = (s + PD <= 18 ? PD : 18 - s) - 1;
        asm volatile("s_waitcnt vmcnt(%c1)" : "+v"(ofr[s]) : "i"(young) : "memory"); }
      GAP();
      mfma_vv(oc[n % 5], wf[n], __builtin_bit_cast(bf16x8, ofr[n / 5]));
      if constexpr (n % 5 == 1 && n / 5 + PD < 18) oload(std::integral_constant<int, n / 5 + PD>{});
      if constexpr (n == 86) { if (b == 0 && w == 7) {
#pragma unroll
        for (int i = 0; i < 4; ++i) { const int idx = 64 * i + lane; ld8_sc1(epf[i], ws + W_OF + (idx >> 3) * 8 * 128 + (idx & 7) * 8); } } }
      GAP();
    });
    asm volatile("s_nop 7" : "+v"(oc[0]), "+v"(oc[1]), "+v"(oc[2]), "+v"(oc[3]), "+v"(oc[4]));
    if (b == 0 && w == 7) asm volatile("s_waitcnt vmcnt(0)" : "+v"(epf[0]), "+v"(epf[1]), "+v"(epf[2]), "+v"(epf[3]) :: "memory");
    STAMP(9);
    char* orow = out + (size_t)(tt * 128 + 16 * w + t16) * 2304 + pb * 1152 + hg * 144;
#pragma unroll
    for (int pr = 0; pr < 2; ++pr) {
      const u32x2 n0 = pack4(oc[2 * pr]), n1 = pack4(oc[2 * pr + 1]);
      auto r0 = __builtin_amdgcn_permlane16_swap(n0[0], n1[0], false, false);
      auto r1 = __builtin_amdgcn_permlane16_swap(n0[1], n1[1], false, false);
      const int col = pr * 32 + (g & 1) * 16 + (g >> 1) * 8;
      __builtin_nontemporal_store(u32x4{r0[0], r1[0], r0[1], r1[1]}, (u32x4*)(orow + col * 2));
    }
    if (g < 2) __builtin_nontemporal_store(pack4(oc[4]), (u32x2*)(orow + (64 + 4 * g) * 2));
    if (b == 0 && w == 7) {
      const bool seen = __all(epf[0] == tag && epf[1] == tag && epf[2] == tag && epf[3] == tag);
      if (!seen) {
#pragma unroll
        for (int i = 0; i < 4; ++i) {
          const int idx = 64 * i + lane;
          wait_flags_at((const u64*)(ws + W_OF + (idx >> 3) * 8 * 128 + (idx & 7) * 8), tag);
        }
      }
      if (lane == 0) __hip_atomic_store((GAS u64*)(ws + W_EP), tag, RLX_AGENT);
    }
  }
  STAMP(10);
  asm volatile(".Lfk_end:" ::: "memory");
}

extern "C" void kernel_launch(void* const* d_in, const int* in_sizes, int n_in, void* d_out, int out_size, void* d_ws, size_t ws_size, hipStream_t stream) {
  (void)hipFuncSetAttribute((const void*)fused_kernel, hipFuncAttributeMaxDynamicSharedMemorySize, LDS_BYTES);
  fused_kernel<<<256, 512, LDS_BYTES, stream>>>((const char*)d_in[0], (const char*)d_in[1], (const char*)d_in[2], (const char*)d_in[3], (const char*)d_in[4],
                                                (const char*)d_in[5], (const char*)d_in[6], (char*)d_out, (char*)d_ws);
}
```
